```python
import torch

def _softmax(x, axis=-1):
    return torch.softmax(x, dim=axis)

def _gelu(x):
    return torch.nn.functional.gelu(x, approximate='tanh')

def _silu(x):
    return torch.nn.functional.silu(x)

def _sigmoid(x):
    return torch.sigmoid(x)

def _layernorm(x, axis=-1, eps=1e-05):
    m = x.mean(dim=axis, keepdim=True)
    v = x.var(dim=axis, keepdim=True, unbiased=False)
    return (x - m) / torch.sqrt(v + eps)

def _scatter_add(tgt, idx, src, axis=0):
    return tgt.clone().index_add_(axis, idx, src)

def _segment_sum(src, seg, n):
    out = torch.zeros((n,) + src.shape[1:], dtype=src.dtype)
    return out.index_add_(0, seg, src)

def _pad(x, pw):
    flat = tuple((v for pair in reversed(pw) for v in pair))
    return torch.nn.functional.pad(x, flat)
kernel_name = 'recurrent_scan__memory_aug_recurrent_mem_tr__glu_gated_geglu__prompt'

def setup_inputs():

    _w = lambda a: (lambda t: t.to(torch.bfloat16) if t.is_floating_point() else t)(torch.as_tensor(a))

    def _A_setup():
        B, S, Dm = (1, 2048, 512)
        H, Dh = (32, 128)
        M, seg = (8, 32)
        seg = min(seg, S)
        S = S // seg * seg
        return {'x': _w((lambda *s: torch.randn(*s, dtype=torch.bfloat16) * 0.1)(B, S, Dm)), 'mem0': _w((lambda *s: torch.randn(*s, dtype=torch.bfloat16) * 1.6)(B, M, Dm)), 'Wq': _w((lambda *s: torch.randn(*s, dtype=torch.bfloat16) * 0.8)(Dm, H, Dh) * Dm ** (-0.5)), 'Wk': _w((lambda *s: torch.randn(*s, dtype=torch.bfloat16) * 0.8)(Dm, H, Dh) * Dm ** (-0.5)), 'Wv': _w((lambda *s: torch.randn(*s, dtype=torch.bfloat16) * 1.6)(Dm, H, Dh) * Dm ** (-0.5)), 'Wo': _w((lambda *s: torch.randn(*s, dtype=torch.bfloat16) * 1.6)(H, Dh, Dm) * (H * Dh) ** (-0.5)), 'seg_len': seg}
    _A_in = _A_setup()

    def _B_setup():
        B, S, Dm, Df = (1, 2048, 512, 1792)
        return {'x': _w((lambda *s: torch.randn(*s, dtype=torch.bfloat16) * 0.1)(B, S, Dm)), 'w_gate': _w((lambda *s: torch.randn(*s, dtype=torch.bfloat16) * 0.4)(Dm, Df) * Dm ** (-0.5)), 'w_up': _w((lambda *s: torch.randn(*s, dtype=torch.bfloat16) * 0.4)(Dm, Df) * Dm ** (-0.5)), 'w_down': _w((lambda *s: torch.randn(*s, dtype=torch.bfloat16) * 0.4)(Df, Dm) * Df ** (-0.5))}
    _B_in = _B_setup()
    return {'A_x': _A_in['x'], 'A_mem0': _A_in['mem0'], 'A_Wq': _A_in['Wq'], 'A_Wk': _A_in['Wk'], 'A_Wv': _A_in['Wv'], 'A_Wo': _A_in['Wo'], 'A_seg_len': _A_in['seg_len'], 'B_x': _B_in['x'], 'B_w_gate': _B_in['w_gate'], 'B_w_up': _B_in['w_up'], 'B_w_down': _B_in['w_down']}

def reference(A_x, A_mem0, A_Wq, A_Wk, A_Wv, A_Wo, A_seg_len, B_x, B_w_gate, B_w_up, B_w_down):

    def _A_ref(x, mem0, Wq, Wk, Wv, Wo, seg_len):
        B, S, Dm = x.shape
        H, Dh = (Wq.shape[1], Wq.shape[2])
        M = mem0.shape[1]
        scale = Dh ** (-0.5)
        n_seg = S // seg_len
        L = M + seg_len
        idx = torch.arange(L)
        mask = (idx[:, None] < M) | (idx[:, None] >= idx[None, :])
        mem = mem0
        outs = []
        for s in range(n_seg):
            xs = x[:, s * seg_len:(s + 1) * seg_len, :].clone()
            h = torch.concatenate([mem, xs], axis=1)
            q = torch.einsum('btd,dhe->bhte', h, Wq)
            k = torch.einsum('btd,dhe->bhte', h, Wk)
            v = torch.einsum('btd,dhe->bhte', h, Wv)
            scores = torch.einsum('bhqe,bhke->bhqk', q, k) * scale
            scores = torch.where(mask[None, None], scores, -1000000000.0)
            p = _softmax(scores, axis=-1)
            o = torch.einsum('bhqk,bhke->bhqe', p, v)
            o = torch.einsum('bhte,hed->btd', o, Wo)
            mem = o[:, :M, :].clone()
            outs.append(o[:, M:, :])
        return torch.concatenate(outs, axis=1)

    def _B_ref(x, w_gate, w_up, w_down):
        g = torch.einsum('bsd,df->bsf', x, w_gate)
        u = torch.einsum('bsd,df->bsf', x, w_up)
        h = _gelu(g) * u
        return torch.einsum('bsf,fd->bsd', h, w_down)
    x = A_x
    xn = _layernorm(x, axis=-1)
    s = _A_ref(xn, A_mem0, A_Wq, A_Wk, A_Wv, A_Wo, A_seg_len)
    g = _silu(_B_ref(xn, B_w_gate, B_w_up, B_w_down))
    return x + s * g
```

```cpp
#include <hip/hip_runtime.h>
#include <hip/hip_bf16.h>
typedef unsigned short u16;
__device__ __forceinline__ float b2f(u16 v) { return __uint_as_float((unsigned)v << 16); }
__device__ __forceinline__ u16 f2b(float f) { __hip_bfloat16 b = __float2bfloat16(f); return *(u16*)&b; }
constexpr int S = 2048, DM = 512, H = 32, DH = 128, M = 8, SEG = 32, NSEG = 64, DF = 1792;

__global__ void ln_k(const u16* x, u16* xn) {
    int row = blockIdx.x, t = threadIdx.x;
    float v[8]; float s = 0;
    for (int i = 0; i < 8; ++i) { v[i] = b2f(x[row * DM + t * 8 + i]); s += v[i]; }
    for (int o = 32; o; o >>= 1) s += __shfl_xor(s, o);
    float m = s / DM; float q = 0;
    for (int i = 0; i < 8; ++i) q += (v[i] - m) * (v[i] - m);
    for (int o = 32; o; o >>= 1) q += __shfl_xor(q, o);
    float r = 1.0f / sqrtf(q / DM + 1e-5f);
    for (int i = 0; i < 8; ++i) xn[row * DM + t * 8 + i] = f2b((v[i] - m) * r);
}
__global__ void gemm_k(const u16* A, const u16* B, u16* C, int Mr, int N, int K) {
    __shared__ float sa[16][65], sb[16][65];
    int tx = threadIdx.x & 15, ty = threadIdx.x >> 4; int m0 = blockIdx.y * 64, n0 = blockIdx.x * 64;
    float acc[4][4] = {};
    for (int k0 = 0; k0 < K; k0 += 16) {
        for (int i = threadIdx.x; i < 1024; i += 256) { int r = i >> 4, c = i & 15; sa[c][r] = (m0 + r < Mr) ? b2f(A[(size_t)(m0 + r) * K + k0 + c]) : 0.f; }
        for (int i = threadIdx.x; i < 1024; i += 256) { int r = i >> 6, c = i & 63; sb[r][c] = b2f(B[(size_t)(k0 + r) * N + n0 + c]); }
        __syncthreads();
        for (int k = 0; k < 16; ++k) { float a[4], b[4]; for (int i = 0; i < 4; ++i) { a[i] = sa[k][ty * 4 + i]; b[i] = sb[k][tx * 4 + i]; }
            for (int i = 0; i < 4; ++i) for (int j = 0; j < 4; ++j) acc[i][j] += a[i] * b[j]; }
        __syncthreads();
    }
    for (int i = 0; i < 4; ++i) if (m0 + ty * 4 + i < Mr) for (int j = 0; j < 4; ++j) C[(size_t)(m0 + ty * 4 + i) * N + n0 + tx * 4 + j] = f2b(acc[i][j]);
}
__global__ void attn_k(const u16* q, const u16* km, const u16* vm, const u16* kx, const u16* vx, u16* o, int isx) {
    int h = blockIdx.x, r = blockIdx.y, seg = blockIdx.z, t = threadIdx.x;
    __shared__ float p[40];
    const u16* qr = q + ((size_t)seg * gridDim.y + r) * 4096 + h * DH;
    km += (size_t)seg * 8 * 4096; vm += (size_t)seg * 8 * 4096; kx += (size_t)seg * 32 * 4096; vx += (size_t)seg * 32 * 4096;
    if (t < 40) { const u16* kr = (t < 8 ? km + t * 4096 : kx + (t - 8) * 4096) + h * DH; float s = 0;
        for (int d = 0; d < DH; ++d) s += b2f(qr[d]) * b2f(kr[d]);
        s *= 0.08838834764831845f; if (isx && t - 8 > r) s = -1e9f; p[t] = s; }
    __syncthreads();
    float mx = -3e38f; for (int j = 0; j < 40; ++j) mx = fmaxf(mx, p[j]);
    float sum = 0; for (int j = 0; j < 40; ++j) sum += expf(p[j] - mx);
    float acc = 0;
    for (int j = 0; j < 40; ++j) { float w = b2f(f2b(expf(p[j] - mx) / sum)); const u16* vr = (j < 8 ? vm + j * 4096 : vx + (j - 8) * 4096) + h * DH; acc += w * b2f(vr[t]); }
    o[((size_t)seg * gridDim.y + r) * 4096 + h * DH + t] = f2b(acc);
}
__global__ void geglu_k(const u16* g, const u16* u, u16* h, int n) { int i = blockIdx.x * 256 + threadIdx.x; if (i < n) { float x = b2f(g[i]); float ge = 0.5f * x * (1.f + tanhf(0.7978845608028654f * (x + 0.044715f * x * x * x))); h[i] = f2b(b2f(f2b(ge)) * b2f(u[i])); } }
__global__ void fin_k(const u16* x, const u16* s, const u16* y, u16* out, int n) { int i = blockIdx.x * 256 + threadIdx.x; if (i < n) { float yy = b2f(y[i]); float g = b2f(f2b(yy / (1.f + expf(-yy)))); out[i] = f2b(b2f(x[i]) + b2f(f2b(b2f(s[i]) * g))); } }

extern "C" void kernel_launch(void* const* d_in, const int* in_sizes, int n_in, void* d_out, int out_size, void* d_ws, size_t ws_size, hipStream_t st) {
    const u16 *x = (const u16*)d_in[0], *mem0 = (const u16*)d_in[1], *Wq = (const u16*)d_in[2], *Wk = (const u16*)d_in[3], *Wv = (const u16*)d_in[4], *Wo = (const u16*)d_in[5];
    const u16 *Wg = (const u16*)d_in[8], *Wu = (const u16*)d_in[9], *Wd = (const u16*)d_in[10];
    u16* w = (u16*)d_ws; size_t o = 0;
    auto al = [&](size_t n) { u16* p = w + o; o += n; return p; };
    u16 *xn = al(S * DM), *Q = al((size_t)S * 4096), *Kx = al((size_t)S * 4096), *Vx = al((size_t)S * 4096), *O = al((size_t)S * 4096);
    u16 *G = al((size_t)S * DF), *U = al((size_t)S * DF), *Hh = al((size_t)S * DF), *Y = al(S * DM), *Sx = al(S * DM);
    u16 *mem = al(65 * 8 * DM), *qm = al(8 * 4096), *km = al(64 * 8 * 4096), *vm = al(64 * 8 * 4096), *om = al(8 * 4096);
    ln_k<<<S, 64, 0, st>>>(x, xn);
    gemm_k<<<dim3(64, 32), 256, 0, st>>>(xn, Wq, Q, S, 4096, DM);
    gemm_k<<<dim3(64, 32), 256, 0, st>>>(xn, Wk, Kx, S, 4096, DM);
    gemm_k<<<dim3(64, 32), 256, 0, st>>>(xn, Wv, Vx, S, 4096, DM);
    gemm_k<<<dim3(28, 32), 256, 0, st>>>(xn, Wg, G, S, DF, DM);
    gemm_k<<<dim3(28, 32), 256, 0, st>>>(xn, Wu, U, S, DF, DM);
    geglu_k<<<S * DF / 256, 256, 0, st>>>(G, U, Hh, S * DF);
    gemm_k<<<dim3(8, 32), 256, 0, st>>>(Hh, Wd, Y, S, DM, DF);
    hipMemcpyAsync(mem, mem0, 8 * DM * 2, hipMemcpyDeviceToDevice, st);
    for (int s = 0; s < NSEG; ++s) {
        u16* m = mem + s * 8 * DM;
        gemm_k<<<dim3(64, 1), 256, 0, st>>>(m, Wk, km + s * 8 * 4096, 8, 4096, DM);
        gemm_k<<<dim3(64, 1), 256, 0, st>>>(m, Wv, vm + s * 8 * 4096, 8, 4096, DM);
        if (s == NSEG - 1) break;
        gemm_k<<<dim3(64, 1), 256, 0, st>>>(m, Wq, qm, 8, 4096, DM);
        attn_k<<<dim3(H, 8, 1), 128, 0, st>>>(qm, km + s * 8 * 4096, vm + s * 8 * 4096, Kx + (size_t)s * 32 * 4096, Vx + (size_t)s * 32 * 4096, om, 0);
        gemm_k<<<dim3(8, 1), 256, 0, st>>>(om, Wo, m + 8 * DM, 8, DM, 4096);
    }
    attn_k<<<dim3(H, 32, NSEG), 128, 0, st>>>(Q, km, vm, Kx, Vx, O, 1);
    gemm_k<<<dim3(8, 32), 256, 0, st>>>(O, Wo, Sx, S, DM, 4096);
    fin_k<<<S * DM / 256, 256, 0, st>>>(x, Sx, Y, (u16*)d_out, S * DM);
}
```

```cpp
#define FARX 1
#define CLSX 4
#include <hip/hip_runtime.h>
#include <hip/hip_bf16.h>
typedef unsigned short u16;
typedef unsigned u32;
typedef u32 v2u __attribute__((ext_vector_type(2)));
typedef u32 v4u __attribute__((ext_vector_type(4)));
typedef u32 v32u __attribute__((ext_vector_type(32)));
typedef __bf16 bf16x8 __attribute__((ext_vector_type(8)));
typedef __bf16 bf16x4 __attribute__((ext_vector_type(4)));
typedef __bf16 bf16x2 __attribute__((ext_vector_type(2)));
typedef float f32x4 __attribute__((ext_vector_type(4)));
typedef float f32x2 __attribute__((ext_vector_type(2)));
#define LAS __attribute__((address_space(3)))
#define DI __device__ __forceinline__
constexpr u32 SENT = 0xFFBFFFBFu;
constexpr u32 OOB = 0x80000000u;
constexpr u32 B_PART = 0, B_MEM = 2u << 20, B_CB = 4u << 20;
constexpr u32 O_CTL = 8u << 20;
constexpr u32 O_PROBE = O_CTL, O_DBG = 6u << 20, O_KMF = O_CTL + 65536;
constexpr u32 O_OF = O_KMF + 8192, O_HF = O_OF + 8192, O_RDY = O_HF + 8192;
constexpr u32 O_KF = O_RDY + 16384, O_EC = O_KF + 16384;
constexpr u32 O_EPOCH = O_CTL + 2048, O_REC = O_CTL + 4096, O_PRB = O_CTL + 49152;
constexpr u32 O_XN = 9u << 20;
constexpr u32 O_KM = 12u << 20;
constexpr u32 O_KV = 16u << 20;
constexpr u32 O_O = 82u << 20;
constexpr u32 O_H = 114u << 20;
constexpr u32 O_END = 130u << 20;
DI float b2f(u16 v) { return __uint_as_float((u32)v << 16); }
DI u16 f2b(float f) { __bf16 b = (__bf16)f; return __builtin_bit_cast(u16, b); }
DI u32 pk2(float a, float b) { bf16x2 r = {(__bf16)a, (__bf16)b}; return __builtin_bit_cast(u32, r); }
DI u32 amap(u32 o, u32 sel) { u32 a = ((o >> 13) << 15) + (o & 8191u); return a | ((sel & 1u) << 13) | ((((sel >> 1) ^ (a >> 19)) & 1u) << 14); }
DI u32 far(u32 base, u32 o, u32 sel0) { const u32 a = base + ((o >> 13) << 14) + (o & 8191u); return a | (((FARX ^ sel0 ^ (a >> 21)) & 1u) << 13); }
DI u32 cb_off(u32 e, u32 sel) { return B_CB + (((e >> 3) & 7u) << 15) + (sel << 13) + ((e & 7u) << 10); }
DI int mem_col(int ch, int g, int half, int j) { return 256 * (ch & 1) + 128 * (g >> 1) + 32 * (ch >> 1) + 16 * (j & 1) + 8 * (g & 1) + 4 * half + 2 * (j >> 2) + ((j >> 1) & 1); }
constexpr int krow_u(int ch, int bh, int a) { return 256 * (ch & 1) + 128 * bh + 32 * (ch >> 1) + a; }
DI u32 krow_l(int gb, int q) { return 16 * (q & 1) + 4 * (q >> 1) + 8 * (gb >> 1) + 2 * (gb & 1); }
constexpr u32 mem_ch(int ch) { return (ch & 1) * 256 + ((ch >> 1) & 1) * 128 + (ch >> 2) * 4096; }
DI u32 mem_l(u32 l) { return (l >> 3) * 512 + (l & 7) * 16; }
DI u32 part_off(u32 b, u32 q) { return ((b >> 4) << 17) | (q << 15) | ((b & 15u) << 11); }
DI int lane_id() { int l; asm volatile("v_mbcnt_lo_u32_b32 %0, -1, 0\n v_mbcnt_hi_u32_b32 %0, -1, %0" : "=v"(l)); __builtin_assume(l >= 0 && l < 64); return l; }
DI u32 opq(u32 x) { asm volatile("" : "+v"(x)); return x; }
DI u32 uni(u32 x) { return __builtin_amdgcn_readfirstlane(x); }
#define DMANT ""
template <int LOFF, int OFF = 0> DI void dma16(const void* sbase, u32 voff, u32 ldsdst) {
    asm volatile("s_add_u32 m0, %2, %3\n s_nop 0\n global_load_lds_dwordx4 %0, %1 offset:%4" DMANT :: "v"(voff), "s"(sbase), "s"(ldsdst), "n"(LOFF), "n"(OFF) : "scc", "memory");
}
template <int LOFF, int OFF = 0> DI void dma16c(const void* sbase, u32 voff, u32 ldsdst) {
    asm volatile("s_add_u32 m0, %2, %3\n s_nop 0\n global_load_lds_dwordx4 %0, %1 offset:%4 sc1" :: "v"(voff), "s"(sbase), "s"(ldsdst), "n"(LOFF), "n"(OFF) : "scc", "memory");
}
template <int N> DI void wait_vm_bar() { asm volatile("s_waitcnt vmcnt(%0)\n s_barrier" :: "n"(N) : "memory"); }
DI void wait_lgkm_bar() { asm volatile("s_waitcnt lgkmcnt(0)\n s_barrier" ::: "memory"); }
DI v2u trread(u32 addr) { return __builtin_bit_cast(v2u, __builtin_amdgcn_ds_read_tr16_b64_v4bf16((LAS bf16x4*)addr)); }
template <int I> struct IC { static constexpr int value = I; };
template <int B, int E, class F> DI void sfor(F&& f) { if constexpr (B < E) { f(IC<B>{}); sfor<B + 1, E>(f); } }
DI u32 dma_chunk(int w, int l) { return (u32)((l & 15) ^ (((l >> 4) | ((w & 1) << 2)) << 1)); }
template <int NP, int BASE, class F, class S, class V, class C> DI void stage_panels(int w, F&& first, S&& src, V&& voff, C&& consume) {
    auto issue = [&](auto pc) { constexpr int p = decltype(pc)::value; const u32 vo = voff(pc);
        sfor<0, 8>([&](auto n) { constexpr int u = decltype(n)::value >> 1, a = decltype(n)::value & 1;
            dma16<BASE + (p & 3) * 32768 + (32 * u + 4 * a) * 256>(src(pc, IC<u>{}, IC<a>{}), vo, (u32)w * 2048u); }); };
    sfor<0, (NP < 4 ? NP : 4)>(issue);
    first();
    sfor<0, NP>([&](auto pc) { constexpr int p = decltype(pc)::value; constexpr int left = (NP - 1 - p) < 3 ? (NP - 1 - p) : 3;
        wait_vm_bar<left * 8>();
        consume(pc, (u32)(BASE + (p & 3) * 32768));
        wait_lgkm_bar();
        if constexpr (p + 4 < NP) issue(IC<p + 4>{}); });
}
DI u32 tr_addr(int l, int T) { const int G = l >> 4, q = (l >> 2) & 3, p = l & 3; return (u32)((8 * G + q) * 256 + (((2 * (T ^ (q | ((G & 1) << 2)))) + (p >> 1)) << 4) + ((p & 1) << 3)); }
struct Params { const u16 *x, *mem0, *Wq, *Wk, *Wv, *Wo, *Wg, *Wu, *Wd; u16* out; char* ws; u32 wsbytes; int k1, d2, hb, kexp, mg, ka, pad; };
DI __amdgpu_buffer_rsrc_t ws_rsrc(const Params& p) { return __builtin_amdgcn_make_buffer_rsrc(p.ws, 0, p.wsbytes, 0x00020000); }
DI u32 ld32c(__amdgpu_buffer_rsrc_t rs, u32 off) { return __builtin_amdgcn_raw_buffer_load_b32(rs, off, 0, 16); }
#define SPINCAP 200000

struct Boot { unsigned long long ka; u32 b, ta, tb, k, s0, s1, s2; };
DI Params params(Boot& bt) {
#if __HIP_DEVICE_COMPILE__
    return *(const __attribute__((address_space(4))) Params*)bt.ka;
#else
    return Params();
#endif
}
DI v4u ld16c(__amdgpu_buffer_rsrc_t rs, u32 voff, u32 soff) { return __builtin_amdgcn_raw_buffer_load_b128(rs, voff, soff, 16); }
template <int SLEEP, bool NE = false> DI void poll(__amdgpu_buffer_rsrc_t rs, u32 voff, u32 soff, u32 empty, unsigned long long mask) {
    u32 x, n;
    asm volatile("s_mov_b32 %1, %8\n s_nop 4\n"
                 "1: buffer_load_dword %0, %2, %3, %4 offen sc1\n s_waitcnt vmcnt(0)\n "
                 ".if %9\n v_cmp_ne_u32 vcc, %5, %0\n .else\n v_cmp_eq_u32 vcc, %5, %0\n .endif\n s_nop 0\n s_and_b64 vcc, vcc, %6\n s_cbranch_scc0 2f\n"
                 "s_sleep %7\n s_sub_u32 %1, %1, 1\n s_cbranch_scc0 1b\n s_endpgm\n 2:"
                 : "=&v"(x), "=&s"(n) : "v"(voff), "s"(rs), "s"(soff), "s"(empty), "s"(mask), "n"(SLEEP), "n"(SPINCAP), "n"((int)NE) : "vcc", "scc", "memory");
}
DI void boot_tag(__amdgpu_buffer_rsrc_t rs, Boot& bt) { bt.ta = uni(ld32c(rs, O_EPOCH)) + 1u; bt.tb = bt.ta * 0x9E3779B1u; }
DI void boot_flag(__amdgpu_buffer_rsrc_t rs, const Boot& bt, int w, int l, u32 off) {
    wait_vm_bar<0>();
    if (w == 0) __builtin_amdgcn_raw_buffer_store_b64(v2u{bt.ta, bt.tb}, rs, l == 0 ? 0 : OOB, off, 16);
}
DI void boot_sel(Boot& bt, v4u r) {
    const u32 a = r.y, b = r.w;
    u32 q = min(min((a & 0xffffu) << 2, ((a >> 16) << 2) | 1u), min(((b & 0xffffu) << 2) | 2u, ((b >> 16) << 2) | 3u));
    q = min(q, (u32)__builtin_amdgcn_update_dpp(0u, q, 0xb1, 0xf, 0xf, false)); q = min(q, (u32)__builtin_amdgcn_update_dpp(0u, q, 0x4e, 0xf, 0xf, false)) & 3u;
    bt.s0 = __builtin_amdgcn_readlane(q, 4); bt.s1 = __builtin_amdgcn_readlane(q, 8); bt.s2 = __builtin_amdgcn_readlane(q, 12);
}
DI u32 boot_rec(const Boot& bt, int l) { return l >= 4 && l < 16 ? O_PRB + ((l - 4) * 8 + ((CLSX - bt.k) & 7)) * 128 : O_REC; }
template <int NF, bool TAG, bool REC = true> DI void boot_wait(__amdgpu_buffer_rsrc_t rs, Boot& bt, int w, int l, u32 f0 = 0, u32 f1 = 0) {
    v4u r, f; u32 n, k0, e; unsigned long long m;
    if (TAG) bt.ta = bt.tb = 0;
    asm volatile("s_mov_b32 %[n], %[cap]\n s_nop 4\n"
                 "1: buffer_load_dwordx4 v[0:3], %[vr], %[rs], 0 offen sc1\n"
                 ".if %[nf] > 0\n buffer_load_dwordx2 v[4:5], %[f0], %[rs], 0 offen sc1\n .endif\n"
                 ".if %[nf] > 1\n buffer_load_dwordx2 v[6:7], %[f1], %[rs], 0 offen sc1\n .endif\n"
                 ".if %[tag]\n buffer_load_dword %[e], off, %[rs], %[eo] sc1\n s_waitcnt vmcnt(0)\n v_readfirstlane_b32 %[ta], %[e]\n s_nop 1\n s_add_u32 %[ta], %[ta], 1\n s_mul_i32 %[tb], %[ta], 0x9E3779B1\n .endif\n"
                 "s_waitcnt vmcnt(0)\n v_cmp_ne_u32 vcc, %[ta], v0\n v_cmp_ne_u32 %[m], %[tb], v2\n v_readfirstlane_b32 %[k0], v3\n s_nop 1\n s_or_b64 vcc, vcc, %[m]\n"
                 "s_bitcmp1_b32 vcc_lo, 0\n s_cbranch_scc1 3f\n s_and_b32 %[k0], %[k0], 7\n s_cmp_lg_u32 %[k0], %[k]\n s_cbranch_scc1 4f\n"
                 ".if %[rec]\n s_cmp_lg_u64 vcc, 0\n s_cbranch_scc1 3f\n .endif\n"
                 ".if %[nf] > 0\n v_cmp_ne_u32 vcc, %[ta], v4\n v_cmp_ne_u32 %[m], %[tb], v5\n s_nop 1\n s_or_b64 vcc, vcc, %[m]\n s_cbranch_scc1 3f\n .endif\n"
                 ".if %[nf] > 1\n v_cmp_ne_u32 vcc, %[ta], v6\n v_cmp_ne_u32 %[m], %[tb], v7\n s_nop 1\n s_or_b64 vcc, vcc, %[m]\n s_cbranch_scc1 3f\n .endif\n s_branch 2f\n"
                 "3: s_sleep 5\n s_sub_u32 %[n], %[n], 1\n s_cbranch_scc0 1b\n s_endpgm\n"
                 "4: s_lshl_b32 %[k0], %[k0], 17\n s_or_b32 %[k0], %[k0], %[b]\n s_bitset1_b32 %[k0], 16\n s_mov_b64 %[m], %[ka]\n s_mov_b32 vcc_lo, %[w]\n s_mov_b32 vcc_hi, %[k0]\n"
                 "s_mov_b64 s[0:1], %[m]\n s_mov_b32 s2, vcc_hi\n s_mov_b32 s3, vcc_lo\n s_waitcnt lgkmcnt(0)\n s_barrier\n s_branch Lmain_entry\n 2:"
                 : "=&{v[0:3]}"(r), "=&{v[4:7]}"(f), [n] "=&s"(n), [k0] "=&s"(k0), [m] "=&s"(m), [e] "=&v"(e), [ta] "+&s"(bt.ta), [tb] "+&s"(bt.tb)
                 : [vr] "v"(boot_rec(bt, l)), [f0] "v"(f0), [f1] "v"(f1), [rs] "s"(rs), [eo] "s"(O_EPOCH), [k] "s"(bt.k), [b] "s"(bt.b), [ka] "s"(bt.ka), [w] "s"(w),
                   [cap] "n"(SPINCAP), [nf] "n"(NF), [tag] "n"((int)TAG), [rec] "n"((int)REC) : "vcc", "scc", "memory");
    boot_sel(bt, r);
}

#define PLACE 0
#define PREF 0
#define WRONGK 0
#define HCHK 14
#define NPROBE 8
#define SIDE 0
#define RPREF 3
#define QFIRST 1
#define RSLP 110
#define ILV 3
#define NOBULK 0
#define SWP 7
#define ESLP 8
#define QSLP 4
#define HLOOK 12
#define CPREF 1
#define SWAP 0
#define EW 4
#define EDELAY 0
#define F1DELAY 0
typedef __bf16 bf16x16 __attribute__((ext_vector_type(16)));
typedef u32 v8u __attribute__((ext_vector_type(8)));
#define BST(it, k)
DI bf16x8 as8(v4u x) { return __builtin_bit_cast(bf16x8, x); }
DI bf16x8 half8(const v8u& x, int b) { return as8(b ? v4u{x[4], x[5], x[6], x[7]} : v4u{x[0], x[1], x[2], x[3]}); }
DI f32x4 mma(bf16x8 a, bf16x8 b, f32x4 c) { return __builtin_amdgcn_mfma_f32_16x16x32_bf16(a, b, c, 0, 0, 0); }
DI v4u pack8(f32x4 a, f32x4 b) { return v4u{pk2(a[0], a[1]), pk2(a[2], a[3]), pk2(b[0], b[1]), pk2(b[2], b[3])}; }
template <class F> DI float xr16(float x, F f) {
    u32 u = __float_as_uint(x); auto r = __builtin_amdgcn_permlane32_swap(u, u, false, false); x = f(__uint_as_float(r[0]), __uint_as_float(r[1]));
    u = __float_as_uint(x); auto q = __builtin_amdgcn_permlane16_swap(u, u, false, false); return f(__uint_as_float(q[0]), __uint_as_float(q[1])); }
DI v4u lds16(u32 a) { return *(LAS v4u*)a; }
DI void st16c(__amdgpu_buffer_rsrc_t rs, u32 voff, u32 soff, v4u x) {
    __builtin_amdgcn_raw_buffer_store_b128(x, rs, voff, soff, 16);
    asm volatile("s_nop 1" :: "v"(x) : "memory");
}
DI void publish(__amdgpu_buffer_rsrc_t rs, u32 off, int w, int l) {
    wait_vm_bar<0>();
    if (w == 0) __builtin_amdgcn_raw_buffer_store_b32(1u, rs, l == 0 ? 0 : OOB, off, 16);
}
DI float sum8(float x) {
    x += __uint_as_float(__builtin_amdgcn_update_dpp(0u, __float_as_uint(x), 0x128, 0xf, 0xf, false));
    return xr16(x, [](float a, float b) { return a + b; }); }
DI const v4u* ln_src(const Params& p, int s, int w, int l) { return (const v4u*)(p.x + (s * 32 + w * 8 + (l & 7)) * 512 + (l >> 3) * 64); }
template <class S> DI void layernorm8(const v4u (&raw)[8], int w, int l, S&& store) {
    const int i = l & 7, q = l >> 3, n0 = 8 * (q & 1) + 2 * (q >> 2) + ((q >> 1) & 1);
    float f[64]; float sum = 0;
    for (int k = 0; k < 32; ++k) { const u32 d = raw[k >> 2][k & 3]; f[2 * k] = __uint_as_float(d << 16); f[2 * k + 1] = __uint_as_float(d & 0xffff0000u); sum += f[2 * k] + f[2 * k + 1]; }
    const float m = sum8(sum) * (1.f / 512); float qq = 0;
    for (int k = 0; k < 64; ++k) { f[k] -= m; qq += f[k] * f[k]; }
    const float r = 1.0f / sqrtf(sum8(qq) * (1.f / 512) + 1e-5f);
    for (int u = 0; u < 8; ++u) { const int e = u >> 2, gb = u & 3, c = 32 * e + 8 * (gb >> 1) + 2 * (gb & 1);
        const v4u o = {pk2(f[c] * r, f[c + 16] * r), pk2(f[c + 4] * r, f[c + 20] * r), pk2(f[c + 1] * r, f[c + 17] * r), pk2(f[c + 5] * r, f[c + 21] * r)};
        store(n0 * 2048 + i * 16 + gb * 256, (w >> 1) * 1024 + (w & 1) * 128 + e * 8192, o); }
}
DI void ln_segment(const Params& p, __amdgpu_buffer_rsrc_t rs, int s, int w, int l) {
    v4u raw[8]; const v4u* src = ln_src(p, s, w, l); for (int k = 0; k < 8; ++k) raw[k] = src[k];
    layernorm8(raw, w, l, [&](u32 vo, u32 so, v4u o) { st16c(rs, vo, O_XN + s * 32768 + so, o); });
}
template <int N, int STEP> DI void touch(const char* base, u32 voff) {
    for (int n = 0; n < N; ++n) asm volatile("s_mov_b32 m0, %2\n s_nop 0\n global_load_lds_dword %0, %1" :: "v"(voff), "s"(base + (size_t)n * STEP), "n"(163840 - 256) : "memory");
}
template <int NM, int LD, class H0, class H> DI void load_cols(v8u (&W)[NM][2][8], const char* const (&m)[NM], int w, int l, H0&& first, H&& hook) {
    const int g = l >> 4;
    const u32 vA = krow_l(w, g) * LD + dma_chunk(w, l) * 16;
    stage_panels<4 * NM, 0>(w, first,
        [&](auto pc, auto uc, auto ac) -> const void* { constexpr int P = decltype(pc)::value, u = decltype(uc)::value, a = decltype(ac)::value, ch = 2 * (P & 3) + (u >> 1);
            return m[P >> 2] + krow_u(ch, u & 1, a) * LD; },
        [&](auto) { return vA; },
        [&](auto pc, u32 base) { constexpr int P = decltype(pc)::value;
            if constexpr (P == 4 * NM - 3) hook();
            sfor<0, 16>([&](auto n) { constexpr int t = decltype(n)::value >> 3, u = (decltype(n)::value >> 1) & 3, a = decltype(n)::value & 1;
                v2u r = trread(tr_addr(l, 2 * w + t) + base + (32 * u + 4 * a) * 256); constexpr int ch = 2 * (P & 3) + (u >> 1), e = 4 * (u & 1) + 2 * a;
                W[P >> 2][t][ch][e] = r.x; W[P >> 2][t][ch][e + 1] = r.y; }); });
}
DI void dma_xn(const Params& p, int s, u32 dst, int w, int l) {
    const char* src = p.ws + O_XN + s * 32768 + w * 8192;
    sfor<0, 8>([&](auto n) { constexpr int k = decltype(n)::value; dma16c<(k >> 2) * 4096, (k & 3) * 1024>(src + (k >> 2) * 4096, l * 16, dst + w * 8192); });
}
DI void role_qkv(Boot& bt, const int w, int h, int par) {
    const Params p = params(bt);
    const int l = lane_id();
    const __amdgpu_buffer_rsrc_t rs = ws_rsrc(p);
    constexpr u32 XB = 131072, XB2 = 98304, PARK = 0, KI = 73728, VI = 78336;
    v8u W[2][2][8], Wq[1][2][8];
    const char* const mats[2] = {(const char*)p.Wk + h * 256, (const char*)p.Wv + h * 256}; const char* const matq[1] = {(const char*)p.Wq + h * 256};
    auto project = [&](auto kvc, auto qc, u32 xb, v4u (&kf)[2], v4u (&vf)[2], v4u (&qf)[2], auto&& mid) { constexpr bool KV = decltype(kvc)::value, Q = decltype(qc)::value;
        f32x4 acc[3][2][2] = {};
        const u32 xa = opq(xb + opq(l) * 16);
        v4u xn0 = lds16(xa), xn1 = lds16(xa + 1024);
        sfor<0, 16>([&](auto nc) { constexpr int n = decltype(nc)::value;
            const bf16x8 x0 = as8(xn0), x1 = as8(xn1);
            if constexpr (n < 15) { xn0 = lds16(xa + (n + 1) * 2048); xn1 = lds16(xa + (n + 1) * 2048 + 1024); }
            if constexpr (n == 4) mid();
            __builtin_amdgcn_sched_barrier(0);
            sfor<0, 2>([&](auto tc) { constexpr int t = decltype(tc)::value;
                if constexpr (KV) { const bf16x8 wk = half8(W[0][t][n >> 1], n & 1), wv = half8(W[1][t][n >> 1], n & 1);
                    acc[0][t][0] = mma(wk, x0, acc[0][t][0]); acc[0][t][1] = mma(wk, x1, acc[0][t][1]);
                    acc[1][t][0] = mma(x0, wv, acc[1][t][0]); acc[1][t][1] = mma(x1, wv, acc[1][t][1]); }
                if constexpr (Q) { const bf16x8 wq = half8(Wq[0][t][n >> 1], n & 1);
                    acc[2][t][0] = mma(wq, x0, acc[2][t][0]); acc[2][t][1] = mma(wq, x1, acc[2][t][1]); } });
            __builtin_amdgcn_sched_barrier(0); });
        for (int t = 0; t < 2; ++t) { if (KV) { kf[t] = pack8(acc[0][0][t], acc[0][1][t]); vf[t] = pack8(acc[1][t][0], acc[1][t][1]); } if (Q) qf[t] = pack8(acc[2][0][t], acc[2][1][t]); }
    };
    auto send = [&](int s, const v4u (&kf)[2], const v4u (&vf)[2]) { const u32 kv = far(O_KV, (s * 32 + h) * 16384, bt.s0), l16 = opq(l) * 16;
        for (int t = 0; t < 2; ++t) { st16c(rs, l16, kv + (t * 4 + w) * 1024, kf[t]); st16c(rs, l16, kv + 16384 + (2 * w + t) * 1024, vf[t]); } };
    auto keep = [&](u32 park, const v4u (&kf)[2], const v4u (&vf)[2], const v4u (&qf)[2]) { const u32 l16 = opq(l) * 16, pa = opq(park + w * 1024 + l16), pv = opq(park + 8192 + w * 2048 + l16);
        for (int t = 0; t < 2; ++t) { *(LAS v4u*)(pa + t * 4096) = kf[t]; *(LAS v4u*)(pv + t * 1024) = vf[t]; *(LAS v4u*)(pa + 16384 + t * 4096) = qf[t]; }
        asm volatile("s_waitcnt lgkmcnt(0)" ::: "memory"); };
    BST(64, 0);
    {
        v4u kf[2], vf[2], qf[2];
        load_cols<2, 8192>(W, mats, w, l, [] {}, [&] { boot_wait<1, true>(rs, bt, w, l, O_RDY + (64 + par + 3 * min(l, 2)) * 128); dma_xn(p, par, XB, w, l); });
        dma_xn(p, par + 3, 0, w, l); dma_xn(p, par + 6, 32768, w, l);
        BST(64, 1);
        auto kvflag = [&](int s) { if (w == 0) __builtin_amdgcn_raw_buffer_store_b32(1u, rs, l == 0 ? 0 : OOB, cb_off(s, bt.s2) + 8 + h * 32, 16); };
        sfor<0, 3>([&](auto ec) { constexpr int e = decltype(ec)::value; BST(par + 3 * e, 0);
            project(IC<1>{}, IC<0>{}, e ? (e - 1) * 32768 : XB, kf, vf, qf, [] {});
            send(par + 3 * e, kf, vf); BST(par + 3 * e, 1);
            if (e == 0) boot_flag(rs, bt, w, l, O_KF + (4 * h + par) * 128);
            if (e == 1) { boot_wait<1, false>(rs, bt, w, l, O_RDY + l * 128); wait_vm_bar<0>(); kvflag(par); kvflag(par + 3); }
            if (e == 2) { wait_vm_bar<0>(); kvflag(par + 6); }
            BST(par + 3 * e, 2); });
        load_cols<1, 8192>(Wq, matq, w, l, [] {}, [] {});
        dma_xn(p, par + 3, XB - 32768, w, l);
        for (int e = 0; e < 3; ++e) { const char* src = p.ws + far(O_KV, ((par + 3 * e) * 32 + h) * 16384 + w * 4096, bt.s0);
            sfor<0, 4>([&](auto n) { dma16c<0, decltype(n)::value * 1024>(src, l * 16, PARK + e * 24576 + w * 4096); }); }
        sfor<0, 3>([&](auto ec) { constexpr int e = decltype(ec)::value;
            project(IC<0>{}, IC<1>{}, e == 1 ? XB - 32768 : XB, kf, vf, qf, [] {});
            const u32 pa = opq(PARK + e * 24576 + 16384 + w * 1024 + l * 16); *(LAS v4u*)pa = qf[0]; *(LAS v4u*)(pa + 4096) = qf[1];
            asm volatile("s_waitcnt lgkmcnt(0)" ::: "memory"); wait_vm_bar<0>();
            if (e != 1) dma_xn(p, par + 6 + 3 * (e >> 1), XB, w, l); });
    }
    const u32 s1 = bt.s1, s2 = bt.s2;
    u32 hint = 1;
    auto p1 = [&](int s, u32 park, u32 xb, u32 flag) {
        BST(s, 0);
        v4u kf[2], vf[2], qf[2];
        project(IC<1>{}, IC<1>{}, xb, kf, vf, qf, [&] { publish(rs, flag, w, l); if (s + 3 < 64) dma_xn(p, s + 3, xb ^ (XB ^ XB2), w, l); });
        const u32 hv = ld32c(rs, O_KMF + h * 256 + (s - 6) * 4);
        send(s, kf, vf); keep(park, kf, vf, qf); BST(s, 1);
        hint = uni(hv);
    };
    auto p2 = [&](int s, u32 park) {
        const int l = lane_id(), i = l & 15, g = l >> 4, qt = w & 1, fh = w >> 1;
        f32x4 t0 = {}, t1 = {}, t2 = {}; v4u q[4];
        { const u32 pa = opq(park + l * 16), qa = opq(park + 16384 + qt * 4096 + l * 16); v4u k1[4], k2[4];
          for (int ks = 0; ks < 4; ++ks) { q[ks] = lds16(qa + ks * 1024); k1[ks] = lds16(pa + ks * 1024); k2[ks] = lds16(pa + 4096 + ks * 1024); }
          __builtin_amdgcn_sched_barrier(0);
          for (int ks = 0; ks < 4; ++ks) { t1 = mma(as8(k1[ks]), as8(q[ks]), t1); t2 = mma(as8(k2[ks]), as8(q[ks]), t2); } }
        const float sc = 0.08838834764831845f, L2E = 1.4426950408889634f; const int qi = 16 * qt + i;
        float e[10];
        for (int r = 0; r < 4; ++r) { e[2 + r] = (4 * g + r <= qi) ? t1[r] * sc : -1e9f; e[6 + r] = (16 + 4 * g + r <= qi) ? t2[r] * sc : -1e9f; }
        for (int r = 2; r < 10; ++r) asm volatile("" : "+v"(e[r]));
        for (int ks = 0; ks < 4; ++ks) asm volatile("" : "+a"(q[ks]));
        if (s > 60) poll<1>(rs, 0, O_KMF + h * 256 + s * 4, 0, 1); else if (!hint) poll<QSLP>(rs, 0, O_KMF + h * 256 + s * 4, 0, 1);
        BST(s, 3);
        const u32 mo = uni(B_MEM + amap(s * 8192, s1)); const int idx = (i & 2) ? 0xEEEE : 0x4444;
        f32x4 a[2][2];
        for (int sp = 0;; ++sp) { u32 mx = 0; v4u m[8];
            for (int c = 0; c < 8; ++c) { m[c] = ld16c(rs, mem_l(l) + mem_ch(c & 3), mo + (c >> 2) * 4096); asm volatile("s_nop 7" ::: "memory"); }
            for (int c = 0; c < 4; ++c) { a[c >> 1][c & 1] = f32x4{}; asm volatile("" : "+a"(a[c >> 1][c & 1])); }
            __builtin_amdgcn_sched_barrier(0);
            sfor<0, 8>([&](auto cc) { constexpr int c = decltype(cc)::value;
                sfor<0, 4>([&](auto n) { constexpr int mm = decltype(n)::value >> 1, t = decltype(n)::value & 1;
                    a[mm][t] = __builtin_amdgcn_smfmac_f32_16x16x64_bf16(as8(m[c]), __builtin_bit_cast(bf16x16, W[mm][t][c]), a[mm][t], idx, 0, 0); });
                mx = max(mx, max(max(m[c].x, m[c].y), max(m[c].z, m[c].w)));
                __builtin_amdgcn_sched_barrier(0); });
            if (!__any(mx == SENT)) break;
            if (sp > SPINCAP) asm volatile("s_endpgm");
            __builtin_amdgcn_s_sleep(2); asm volatile("" ::: "memory"); }
        BST(s, 4);
        for (int t = 0; t < 2; ++t) { const u32 kk = pk2(a[0][t][0] + a[0][t][2], a[0][t][1] + a[0][t][3]); *(LAS v2u*)(KI + g * 1088 + (32 * w + 16 * t + i) * 8) = v2u{kk, kk}; }
        *(LAS v2u*)(VI + w * 512 + l * 8) = v2u{pk2(a[1][0][0] + a[1][0][2], a[1][0][1] + a[1][0][3]), pk2(a[1][1][0] + a[1][1][2], a[1][1][1] + a[1][1][3])};
        wait_lgkm_bar();
        if (s >= 3 && s + 6 < 64 && w == 0) __builtin_amdgcn_raw_buffer_store_b32(1u, rs, l == 0 ? 0 : OOB, cb_off(s + 6, s2) + 8 + h * 32, 16);
        v4u km[4], vx[4]; v2u vm[2];
        { const u32 ka = opq(KI + (l & 3) * 1088 + (4 * g + ((l >> 2) & 3)) * 8), va = opq(park + 8192 + fh * 4096 + l * 16), vb = opq(VI + fh * 1024 + l * 8);
          for (int ks = 0; ks < 4; ++ks) { const v2u a0 = trread(ka + ks * 256), a1 = trread(ka + ks * 256 + 128); km[ks] = v4u{a0.x, a0.y, a1.x, a1.y}; }
          vm[0] = *(LAS v2u*)vb; vm[1] = *(LAS v2u*)(vb + 512);
          for (int c = 0; c < 4; ++c) vx[c] = lds16(va + c * 1024); }
        __builtin_amdgcn_sched_barrier(0);
        for (int ks = 0; ks < 4; ++ks) t0 = mma(as8(km[ks]), as8(q[ks]), t0);
        e[0] = t0[0] * sc; e[1] = t0[1] * sc;
        float mx = e[0]; for (int r = 1; r < 10; ++r) mx = fmaxf(mx, e[r]);
        mx = xr16(mx, [](float a, float b) { return fmaxf(a, b); });
        float sum = 0; for (int r = 0; r < 10; ++r) { e[r] = __builtin_amdgcn_exp2f((e[r] - mx) * L2E); sum += e[r]; }
        sum = xr16(sum, [](float a, float b) { return a + b; });
        const float inv = __builtin_amdgcn_rcpf(sum);
        const v4u px = {pk2(e[2] * inv, e[3] * inv), pk2(e[4] * inv, e[5] * inv), pk2(e[6] * inv, e[7] * inv), pk2(e[8] * inv, e[9] * inv)}; const u32 pm = pk2(e[0] * inv, e[1] * inv);
        for (int u = 0; u < 2; ++u) { const int wv = 2 * fh + u; f32x4 o[2];
            for (int t = 0; t < 2; ++t) { o[t] = mma(as8(v4u{vm[u][t], 0, 0, 0}), as8(v4u{pm, 0, 0, 0}), f32x4{}); o[t] = mma(as8(vx[2 * u + t]), as8(px), o[t]); }
            st16c(rs, l * 16, far(O_O, ((s * 2 + qt) * 128 + h * 4 + wv) * 1024, bt.s0), pack8(o[0], o[1])); }
    };
    u32 park = PARK, xb = XB; hint = par == 0;
    for (int s = par; s < 64; s += 3) {
        p2(s, park); BST(s, 5);
        const u32 of = O_OF + h * 256 + s * 4; hint = 0;
        if (s + 9 < 64) p1(s + 9, park, xb, of); else publish(rs, of, w, l);
        BST(s, 6);
        xb ^= XB ^ XB2;
        park = park == PARK + 49152 ? PARK : park + 24576;
    }
    __builtin_amdgcn_endpgm();
}
DI void role_f1(Boot& bt, const int w, int fb, int par) {
    const Params p = params(bt);
    const int l = lane_id();
    const __amdgpu_buffer_rsrc_t rs = ws_rsrc(p);
    v8u W[2][2][8];
    const char* const mats[2] = {(const char*)p.Wg + fb * 256, (const char*)p.Wu + fb * 256};
    BST(64, 0);
    { const int j = 4 + 2 * fb + par; ln_segment(p, rs, j, w, l); boot_wait<0, true, false>(rs, bt, w, l); if (j < 9) boot_flag(rs, bt, w, l, O_RDY + (64 + j) * 128);
      ln_segment(p, rs, j + 32, w, l); boot_flag(rs, bt, w, l, O_RDY + (32 + j) * 128); BST(64, 2); }
    for (int k = 0; k < F1DELAY; ++k) __builtin_amdgcn_s_sleep(32);
    load_cols<2, 3584>(W, mats, w, l, [] {}, [&] { boot_wait<1, false>(rs, bt, w, l, O_RDY + l * 128); }); BST(64, 1);
    const int hk = (fb * 4 + w) / 14 * 16 + (fb * 4 + w) % 14;
    u32 xb = 0; dma_xn(p, par, xb, w, l);
    for (int s = par; s < 64; s += 2) {
        wait_vm_bar<0>();
        if (s > par && w == 0) __builtin_amdgcn_raw_buffer_store_b32(1u, rs, l == 0 ? 0 : OOB, O_HF + fb * 256 + (s - 2) * 4, 16);
        BST(s, 0); if (s + 2 < 64) dma_xn(p, s + 2, xb ^ 32768, w, l);
        f32x4 acc[2][2][2] = {};
        const u32 xa = opq(xb + l * 16);
        v4u xn0 = lds16(xa), xn1 = lds16(xa + 1024);
        sfor<0, 16>([&](auto nc) { constexpr int n = decltype(nc)::value;
            const bf16x8 x0 = as8(xn0), x1 = as8(xn1);
            if constexpr (n < 15) { xn0 = lds16(xa + (n + 1) * 2048); xn1 = lds16(xa + (n + 1) * 2048 + 1024); }
            __builtin_amdgcn_sched_barrier(0);
            sfor<0, 4>([&](auto c) { constexpr int m = decltype(c)::value >> 1, t = decltype(c)::value & 1; const bf16x8 wv = half8(W[m][t][n >> 1], n & 1);
                acc[m][t][0] = mma(wv, x0, acc[m][t][0]); acc[m][t][1] = mma(wv, x1, acc[m][t][1]); });
            __builtin_amdgcn_sched_barrier(0); });
        for (int tt = 0; tt < 2; ++tt) { f32x4 hh[2];
            for (int t = 0; t < 2; ++t) for (int r = 0; r < 4; ++r) { const float x = acc[0][t][tt][r];
                const float z = x * (-2.302208198144325f - 0.1029432397f * x * x);
                hh[t][r] = x * __builtin_amdgcn_rcpf(1.f + __builtin_amdgcn_exp2f(z)) * acc[1][t][tt][r]; }
            st16c(rs, l * 16, far(O_H, ((s * 2 + tt) * 64 + hk) * 1024, bt.s0), pack8(hh[0], hh[1])); }
        xb ^= 32768; BST(s, 1);
    }
    publish(rs, O_HF + fb * 256 + (62 + par) * 4, w, l);
    __builtin_amdgcn_endpgm();
}
DI void role_e(Boot& bt, const int w, int n, int tt, int par) {
    const Params p = params(bt);
    const int l = lane_id(), i = l & 15, g = l >> 4;
    const __amdgpu_buffer_rsrc_t rs = ws_rsrc(p);
    v4u W[46][2];
    BST(64, 0);
    for (int k = 0; k < EDELAY; ++k) __builtin_amdgcn_s_sleep(32);
    {
        const char *wd = (const char*)p.Wd + (size_t)w * 14 * 32768 + n * 64, *wo = (const char*)p.Wo + (size_t)w * 32 * 32768 + n * 64;
        const u32 vo = (u32)(l >> 2) * 1024 + (((l & 3) ^ (((l >> 4) & 1) << 1)) << 4), ring = w * 32768;
        const u32 ra = ring + (4 * g + ((l >> 2) & 3)) * 64 + ((l & 1) << 3), c0 = (((l >> 1) & 1) ^ ((g & 1) << 1)) << 4;
        auto issue = [&](auto kc) { constexpr int k = decltype(kc)::value; const char* s = k < 14 ? wd + k * 32768 : wo + (k - 14) * 32768;
            dma16<(k & 15) * 2048>(s, vo, ring); dma16<(k & 15) * 2048 + 1024>(s + 16384, vo, ring); };
        sfor<0, 16>(issue);
        sfor<0, 23>([&](auto gc) { constexpr int k0 = 2 * decltype(gc)::value, left = (44 - k0) < 14 ? (44 - k0) : 14;
            asm volatile("s_waitcnt vmcnt(%0)" :: "n"(2 * left) : "memory");
            sfor<0, 8>([&](auto c) { constexpr int k = k0 + (decltype(c)::value >> 2), t = (decltype(c)::value >> 1) & 1, a = decltype(c)::value & 1;
                v2u r = trread(ra + (c0 ^ (t << 5)) + (k & 15) * 2048 + a * 1024); W[k][t][2 * a] = r.x; W[k][t][2 * a + 1] = r.y; });
            asm volatile("s_waitcnt lgkmcnt(0)" ::: "memory");
            if constexpr (k0 + 16 < 46) { issue(IC<k0 + 16>{}); issue(IC<k0 + 17>{}); } });
    }
    wait_lgkm_bar();
    BST(64, 1);
    boot_wait<1, true>(rs, bt, w, l, O_RDY + l * 128);
    boot_flag(rs, bt, w, l, O_EC + ((par * 2 + tt) * 16 + n) * 128);
    const u32 fo = l < 14 ? O_HF + l * 256 : l < 22 ? O_OF + (8 * w + l - 14) * 256 : OOB;
    u32 fl = __builtin_amdgcn_raw_buffer_load_b32(rs, fo, par * 4, 16);
    for (int s = par; s < 64; s += 2) {
        f32x4 acc[2][2] = {};
        const int tok = s * 32 + tt * 16 + i, col = n * 32 + (w & 1) * 16 + 4 * g + (w >> 1) * 2;
        const unsigned long long have = __ballot(fl != 0);
        if ((have & 0x3fff) != 0x3fff) poll<3>(rs, fo, s * 4, 0, 0x3fff);
        asm volatile("" ::: "memory");
        BST(s, 0);
        v4u a[46]; const u32 rb = (s & 2) * 8192, sh = far(O_H, ((s * 2 + tt) * 64 + 16 * w) * 1024, bt.s0), so = far(O_O, ((s * 2 + tt) * 128 + 32 * w) * 1024, bt.s0);
        const u32 fq = l < 14 ? OOB : fo, f2 = __builtin_amdgcn_raw_buffer_load_b32(rs, fq, s * 4, 16);
        const u32 xr = *(const u32*)(p.x + tok * 512 + col);
        for (int k = 0; k < 14; ++k) a[k] = ld16c(rs, l * 16 + (k & 3) * 1024, sh + (k >> 3) * 16384 + ((k >> 2) & 1) * 4096);
        BST(s, 1);
        __builtin_amdgcn_sched_barrier(0);
        if ((__ballot(f2 != 0) & 0x3fc000) != 0x3fc000) { if (s < 58) poll<ESLP>(rs, fq, s * 4, 0, 0x3fc000); else poll<1>(rs, fq, s * 4, 0, 0x3fc000); }
        asm volatile("" ::: "memory");
        BST(s, 2);
        for (int k = 0; k < EW; ++k) a[14 + k] = ld16c(rs, l * 16 + (k & 3) * 1024, so + (k >> 3) * 16384 + ((k >> 2) & 1) * 4096);
        sfor<0, 46>([&](auto kc) { constexpr int k = decltype(kc)::value, m = k < 14 ? 1 : 0, kn = k + EW;
            __builtin_amdgcn_sched_barrier(0);
            acc[m][0] = mma(as8(W[k][0]), as8(a[k]), acc[m][0]); acc[m][1] = mma(as8(W[k][1]), as8(a[k]), acc[m][1]);
            __builtin_amdgcn_sched_barrier(0);
            if constexpr (kn < 32) a[14 + kn] = ld16c(rs, l * 16 + (kn & 3) * 1024, so + (kn >> 3) * 16384 + ((kn >> 2) & 1) * 4096);
            if constexpr (k == 20) for (int c = 2; c < 4; ++c) *(LAS f32x4*)(rb + (w * 4 + c) * 1024 + l * 16) = acc[1][c & 1];
            if constexpr (k == 40) fl = __builtin_amdgcn_raw_buffer_load_b32(rs, fo, (s + 2) * 4, 16); });
        BST(s, 3);
        for (int c = 0; c < 2; ++c) *(LAS f32x4*)(rb + (w * 4 + c) * 1024 + l * 16) = acc[0][c];
        wait_lgkm_bar();
        { f32x2 sv = {}, yv = {}; const u32 ra = rb + (w & 1) * 1024 + l * 16 + (w >> 1) * 8;
          for (int u = 0; u < 4; ++u) { sv += *(LAS f32x2*)(ra + u * 4096); yv += *(LAS f32x2*)(ra + u * 4096 + 2048); }
          float o[2];
          for (int r = 0; r < 2; ++r) o[r] = __uint_as_float(r ? (xr & 0xffff0000u) : (xr << 16)) + sv[r] * yv[r] * __builtin_amdgcn_rcpf(1.f + __builtin_amdgcn_exp2f(-1.4426950408889634f * yv[r]));
          *(u32*)(p.out + tok * 512 + col) = pk2(o[0], o[1]); }
        if (s == 41 && n == 0 && tt == 0 && w == 3) {
            poll<8, true>(rs, l * 128, O_EC, bt.ta, ~0ull); __builtin_amdgcn_raw_buffer_store_b32(bt.ta, rs, l == 0 ? 0 : OOB, O_EPOCH, 16); }
        BST(s, 4);
    }
    BST(66, 0);
    __builtin_amdgcn_endpgm();
}
DI void role_spare(Boot& bt, const int w, int j) {
    const Params p = params(bt);
    const int l = lane_id(); const __amdgpu_buffer_rsrc_t rs = ws_rsrc(p);
    BST(64, 0);
    ln_segment(p, rs, j, w, l);
    boot_wait<0, true, false>(rs, bt, w, l);
    boot_flag(rs, bt, w, l, O_RDY + (64 + j) * 128);
    BST(64, 1);
    ln_segment(p, rs, j + 32, w, l);
    boot_flag(rs, bt, w, l, O_RDY + (32 + j) * 128);
    __builtin_amdgcn_endpgm();
}

DI void role_head(Boot& bt, const int w, int h, u32 t_entry) {
    const Params p = params(bt);
    const int l = lane_id(), i = l & 15, g = l >> 4;
    const __amdgpu_buffer_rsrc_t rs = ws_rsrc(p);
    constexpr int XIMG = 16384, OIMG = 20992, RING = 24576;
    BST(64, 0);
    u32 a3 = t_entry;
    u32 kv0 = 0;
    v32u W[12];
    {
        const u32 c = dma_chunk(w, l);
        const u32 vA = krow_l(w, g) * 8192u + c * 16;
        const u32 vO = (u32)(32 * (g >> 1) + (g & 1) + 16 * (w & 1) + 4 * (w >> 1)) * 1024u + (c >> 3) * 256 + (c & 7) * 16;
        const char *q = (const char*)p.Wq + h * 256, *k = (const char*)p.Wk + h * 256, *vv = (const char*)p.Wv + h * 256, *o = (const char*)p.Wo + (size_t)h * 131072;
        const long sw = SWAP ? (h & 1) * 256 * 8192 : 0;

        constexpr int PM[16] = {0, 0, 0, 1, 1, 1, 2, 2, 2, 3, 3, 3, 2, 3, 0, 1}, PP[16] = {1, 2, 3, 1, 2, 3, 0, 1, 2, 0, 1, 2, 3, 3, 0, 0};
        stage_panels<16, RING>(w, [] {},
            [&](auto pc, auto uc, auto ac) -> const void* { constexpr int m = PM[decltype(pc)::value], pp = PP[decltype(pc)::value], u = decltype(uc)::value, a = decltype(ac)::value;
                if constexpr (m < 3) { constexpr int ch = 2 * pp + (u >> 1); return (m == 0 ? q : m == 1 ? k : vv) + krow_u(ch, u & 1, a) * 8192 + ((ch & 1) ? -sw : sw); }
                else return o + (64 * (pp >> 1) + 8 * (u & 1) + 2 * a) * 1024 + (u >> 1) * 512 + (pp & 1) * 128; },
            [&](auto pc) { return PM[decltype(pc)::value] < 3 ? vA : vO; },
            [&](auto pc, u32 base) { constexpr int P = decltype(pc)::value, m = PM[P], pp = PP[P];
                if constexpr (P >= 11) BST(65, P - 11);
                if constexpr (P == HLOOK) if (w == 0) dma16c<XIMG>(p.ws, l < 16 ? boot_rec(bt, l) : l == 16 ? O_EPOCH : l < 32 ? O_KF + (4 * h + (l & 1)) * 128 : O_RDY + (l - 32) * 128, 0);
                if constexpr (P == HCHK) {
                    wait_vm_bar<0>();
                    const v4u r = lds16(XIMG + l * 16); const u32 ta = __builtin_amdgcn_readlane(r.x, 16) + 1u, tb = ta * 0x9E3779B1u;
                    if (__all(l == 16 || (r.x == ta && (l < 16 ? r.z : r.y) == tb)) && (uni(r.w) & 7) == bt.k) { bt.ta = ta; bt.tb = tb; boot_sel(bt, r); }
                    else boot_wait<2, true>(rs, bt, w, l, O_RDY + (l & 31) * 128, O_KF + (4 * h + (l & 1)) * 128);
                    BST(65, 5);
                    if (w == 0) __builtin_amdgcn_raw_buffer_store_b32(bt.ta, rs, l == 0 ? 0 : OOB, O_KF + (4 * h + 3) * 128, 16);
                    kv0 = far(O_KV, h * 16384 + w * 2048, bt.s0);
                    dma16c<0>(p.ws + kv0, l * 16, w * 2048); dma16c<0, 1024>(p.ws + kv0, l * 16, w * 2048);
                    dma16c<8192>(p.ws + kv0 + 16384, l * 16, w * 2048); dma16c<8192, 1024>(p.ws + kv0 + 16384, l * 16, w * 2048); }
                auto rd = [&](auto xc, auto bc, auto ac) { constexpr int x = decltype(xc)::value, bh = decltype(bc)::value, a = decltype(ac)::value;
                    if constexpr (m < 3) return trread(tr_addr(l, 2 * w + (x & 1)) + base + (32 * (2 * (x >> 1) + bh) + 4 * a) * 256);
                    else return trread(tr_addr(l, 4 * (w & 1) + x) + base + (u32)(w >> 1) * 16384u + (32 * bh + 4 * a) * 256); };
                if constexpr (P < 12) sfor<0, 16>([&](auto n) { constexpr int x = decltype(n)::value >> 2, bh = (decltype(n)::value >> 1) & 1, a = decltype(n)::value & 1;
                    const v2u r = rd(IC<x>{}, IC<bh>{}, IC<a>{});
                    constexpr int f = m < 2 ? (2 * pp + (x >> 1) - 2) * 4 + 2 * m + (x & 1) : m == 2 ? 24 + 4 * pp + x : 36 + 4 * pp + x;
                    constexpr int e = f * 8 + 4 * bh + 2 * a;
                    W[e >> 5][e & 31] = r.x; W[e >> 5][(e & 31) + 1] = r.y; });
                else {
                    v4u f[8];
                    sfor<0, 16>([&](auto n) { constexpr int x = decltype(n)::value >> 2, bh = (decltype(n)::value >> 1) & 1, a = decltype(n)::value & 1;
                        const v2u r = rd(IC<x>{}, IC<bh>{}, IC<a>{}); f[2 * x + bh][2 * a] = r.x; f[2 * x + bh][2 * a + 1] = r.y; });
                    wait_lgkm_bar();
                    sfor<0, 8>([&](auto n) { *(LAS v4u*)(base + (u32)w * 8192u + decltype(n)::value * 1024 + l * 16) = f[decltype(n)::value]; });
                } });
    }
    const u32 s0 = bt.s0, s1 = bt.s1, s2 = bt.s2;
    BST(64, 1);
    BST(64, 2);
    v4u c0, c1;
    c0.x = l * 16; c0.y = (i & 2) ? 0xEEEEu : 0x4444u; c0.z = XIMG + g * 1088 + (32 * w + i) * 8; c0.w = XIMG + (l & 3) * 1088 + (4 * g + ((l >> 2) & 3)) * 8;
    c1.x = (i & 2) ? OIMG + (w >> 1) * 1024 + (g * 16 + ((i & ~2) | ((w & 1) << 1))) * 16 : OIMG + 2048 + (l | ((~w & 1) << 1)) * 16;
    c1.y = B_PART + amap(part_off(8 * w + 4 * (i >> 3) + g, 0) | (h << 6) | ((i & 7) << 3), s0);
    c1.z = (w == 0 && l == 0) ? h * 32 : OOB; c1.w = RING + w * 8192 + l * 16;
    const u32 c2 = mem_l(l), a31 = SWAP ? (h & 1) * 256 : 0;
    u32 a12 = h, a13 = B_MEM | (s1 << 13), a15 = B_CB | (s2 << 13), a16 = kv0, a17 = w * 2048,
        a18 = p.k1, a58 = p.d2, a35 = p.mg, a64 = p.kexp, a1 = p.ka, a19 = O_DBG + (h * 4 + w) * 8192;
    asm volatile(
    "Lhead_code:\n"
    "s_mov_b32 s24, -1\n"
    "s_mov_b32 s33, 0\n"
    "s_mov_b32 s60, 0\n"
    "s_mov_b32 s61, 0\n"
    "s_mov_b32 s62, 0\n"
    "s_mov_b32 s34, 0x3e0293ee\n"
    "s_mov_b32 s36, 0xbe0293ee\n"
    "s_mov_b32 s38, 0xffbfffbf\n"
    "v_mov_b32 v124, 0x3f803f80\n"
    "v_mov_b32 v125, 0x3f803f80\n"
    "v_mov_b32 v126, 0x3f803f80\n"
    "v_mov_b32 v127, 0x3f803f80\n"
    "s_add_u32 s63, s17, 0x2000\n"
    "s_or_b32 s13, s13, s31\n"
    "s_memtime s[56:57]\n"
    "s_memrealtime s[40:41]\n"
    "s_nop 1\n"
    "s_memrealtime s[42:43]\n"
    "s_nop 1\n"
    "s_memrealtime s[44:45]\n"
    "s_nop 1\n"
    "s_memrealtime s[46:47]\n"
    "s_nop 1\n"
    "s_memrealtime s[48:49]\n"
    "s_nop 1\n"
    "s_memrealtime s[50:51]\n"
    "s_nop 1\n"
    "s_memrealtime s[52:53]\n"
    "s_nop 1\n"
    "s_memrealtime s[54:55]\n"
    "s_waitcnt lgkmcnt(0)\n"
    "s_add_u32 s40, s40, s42\n"
    "s_add_u32 s44, s44, s46\n"
    "s_add_u32 s48, s48, s50\n"
    "s_add_u32 s52, s52, s54\n"
    "s_add_u32 s40, s40, s44\n"
    "s_add_u32 s48, s48, s52\n"
    "s_add_u32 s40, s40, s48\n"
    "s_mul_i32 s40, s40, 3\n"
    "s_sub_u32 s26, s40, s56\n"
    "s_sub_u32 s65, s40, 0x10000\n"
    "s_sub_u32 s14, s40, 0x100000\n"
    "s_mov_b32 s82, s56\n"
    "s_mov_b32 s91, 2300\n"
    "s_mov_b32 s94, s34\n"
    "s_mov_b32 s95, s34\n"
    "v_add_u32 v123, s63, v112\n"
    "ds_read_b128 v[80:83], v123\n"
    "ds_read_b128 v[84:87], v123 offset:1024\n"
    "s_mov_b32 s4, -1\n"
    "s_mov_b32 s24, 0\n"
    "s_mov_b32 s27, s13\n"
    "s_branch LH_go_%=\n"
    ".p2align 6\n"
    "s_nop 0\n"
    "s_nop 0\n"
    "s_nop 0\n"
    "s_nop 0\n"
    "s_nop 0\n"
    "s_nop 0\n"
    "s_nop 0\n"
    "LH_top_%=:\n"
    "s_memtime s[20:21]\n"
    "s_waitcnt lgkmcnt(0)\n"
    "s_sub_i32 s22, s25, s20\n"
    "s_ashr_i32 s22, s22, 3\n"
    "s_max_i32 s22, s22, 0\n"
    "s_cmp_gt_i32 s22, 0x400\n"
    "s_cselect_b32 s22, 0, s22\n"
    "s_cmp_gt_i32 s22, 0x7f\n"
    "s_cbranch_scc1 LH_top_%=\n"
    "s_lshl_b32 s22, s22, 2\n"
    "s_getpc_b64 s[20:21]\n"
    "s_sub_u32 s22, 0x210, s22\n"
    "s_add_u32 s20, s20, s22\n"
    "s_addc_u32 s21, s21, 0\n"
    "s_setpc_b64 s[20:21]\n"
    ".rept 127\n"
    "s_nop 1\n"
    ".endr\n"
    "LH_go_%=:\n"
    "buffer_load_dwordx4 v[0:3], v120, s[8:11], s27 offen offset:0 sc1\n"
    "v_add_u32 v122, 0x10000, v119\n"
    "ds_read_b128 v[88:91], v122 offset:32768\n"
    "v_mov_b64 v[32:33], 0\n"
    "ds_read_b128 v[92:95], v122 offset:33792\n"
    "v_mov_b64 v[34:35], 0\n"
    "s_xor_b32 s98, s27, 0x100\n"
    "s_add_u32 s28, s27, 0x1000\n"
    "s_xor_b32 s99, s28, 0x100\n"
    "buffer_load_dwordx4 v[4:7], v120, s[8:11], s98 offen offset:0 sc1\n"
    "ds_read_b128 v[96:99], v122 offset:0\n"
    "v_mov_b64 v[36:37], 0\n"
    "ds_read_b128 v[100:103], v122 offset:1024\n"
    "v_mov_b64 v[38:39], 0\n"
    "s_and_b32 s29, s24, 1\n"
    "s_lshl_b32 s29, s29, 20\n"
    "s_add_u32 s30, s29, 0x20000\n"
    "s_add_u32 s96, s29, 0x40000\n"
    "buffer_load_dwordx4 v[8:11], v120, s[8:11], s27 offen offset:128 sc1\n"
    "ds_read_b128 v[48:51], v122 offset:34816\n"
    "v_mov_b64 v[40:41], 0\n"
    "ds_read_b128 v[52:55], v122 offset:35840\n"
    "v_mov_b64 v[42:43], 0\n"
    "s_add_u32 s97, s29, 0x60000\n"
    "s_bitcmp1_b32 s24, 0\n"
    "s_cselect_b32 s4, 155648, 0\n"
    "s_cselect_b32 s5, 0, 155648\n"
    "buffer_load_dwordx4 v[12:15], v120, s[8:11], s98 offen offset:128 sc1\n"
    "ds_read_b128 v[56:59], v122 offset:2048\n"
    "v_mov_b64 v[44:45], 0\n"
    "ds_read_b128 v[60:63], v122 offset:3072\n"
    "v_mov_b64 v[46:47], 0\n"
    "s_add_u32 s5, s5, s17\n"
    "s_add_u32 s39, s24, 2\n"
    "s_bfe_u32 s31, s39, 0x30003\n"
    "s_lshl_b32 s31, s31, 15\n"
    "buffer_load_dwordx4 v[16:19], v120, s[8:11], s28 offen offset:0 sc1\n"
    "ds_read_b128 v[64:67], v122 offset:36864\n"
    "v_mov_b64 v[104:105], 0\n"
    "ds_read_b128 v[68:71], v122 offset:37888\n"
    "v_mov_b64 v[106:107], 0\n"
    "s_or_b32 s31, s31, s15\n"
    "s_and_b32 s39, s39, 7\n"
    "s_lshl_b32 s39, s39, 10\n"
    "s_or_b32 s31, s31, s39\n"
    "buffer_load_dwordx4 v[20:23], v120, s[8:11], s99 offen offset:0 sc1\n"
    "ds_read_b128 v[72:75], v122 offset:4096\n"
    "v_mov_b64 v[108:109], 0\n"
    "ds_read_b128 v[76:79], v122 offset:5120\n"
    "v_mov_b64 v[110:111], 0\n"
    "s_add_u32 s59, s24, 1\n"
    "s_lshl_b32 s90, s59, 20\n"
    "s_add_u32 s90, s90, s16\n"
    "s_and_b32 s59, s59, 2\n"
    "buffer_load_dwordx4 v[24:27], v120, s[8:11], s28 offen offset:128 sc1\n"
    "v_add_u32 v121, s4, v112\n"
    "s_lshl_b32 s59, s59, 12\n"
    "s_xor_b32 s90, s90, s59\n"
    "s_add_u32 s59, s90, 0x4000\n"
    "s_sub_u32 s23, s26, s14\n"
    "s_add_u32 s23, s23, s82\n"
    "s_nop 1\n"
    "buffer_load_dwordx4 v[28:31], v120, s[8:11], s99 offen offset:128 sc1\n"
    "s_waitcnt lgkmcnt(0)\n"
    "s_waitcnt vmcnt(7)\n"
    "v_smfmac_f32_16x16x64_bf16 v[40:43], v[0:3], v[88:95], v113\n"
    "ds_read_b128 v[88:91], v122 offset:38912\n"
    "ds_read_b128 v[92:95], v122 offset:39936\n"
    "v_smfmac_f32_16x16x64_bf16 v[32:35], v[0:3], v[96:103], v113\n"
    "ds_read_b128 v[96:99], v122 offset:6144\n"
    "v_smfmac_f32_16x16x64_bf16 v[44:47], v[0:3], v[48:55], v113\n"
    "ds_read_b128 v[100:103], v122 offset:7168\n"
    "v_smfmac_f32_16x16x64_bf16 v[36:39], v[0:3], v[56:63], v113\n"
    "s_mov_b32 m0, s5\n"
    "s_waitcnt vmcnt(6)\n"
    "v_smfmac_f32_16x16x64_bf16 v[40:43], v[4:7], v[64:71], v113\n"
    "v_smfmac_f32_16x16x64_bf16 v[32:35], v[4:7], v[72:79], v113\n"
    "buffer_load_dwordx4 v112, s[8:11], s90 offen offset:0 sc1 lds\n"
    "s_waitcnt lgkmcnt(0)\n"
    "v_smfmac_f32_16x16x64_bf16 v[44:47], v[4:7], v[88:95], v113\n"
    "v_smfmac_f32_16x16x64_bf16 v[36:39], v[4:7], v[96:103], v113\n"
    "s_waitcnt vmcnt(6)\n"
    "v_smfmac_f32_16x16x64_bf16 v[40:43], v[8:11], a[16:23], v113\n"
    "ds_read_b128 v[48:51], v121 offset:0\n"
    "v_smfmac_f32_16x16x64_bf16 v[32:35], v[8:11], a[0:7], v113\n"
    "ds_read_b128 v[52:55], v121 offset:4096\n"
    "v_smfmac_f32_16x16x64_bf16 v[44:47], v[8:11], a[24:31], v113\n"
    "ds_read_b128 v[56:59], v121 offset:1024\n"
    "v_smfmac_f32_16x16x64_bf16 v[36:39], v[8:11], a[8:15], v113\n"
    "ds_read_b128 v[60:63], v121 offset:5120\n"
    "s_waitcnt vmcnt(5)\n"
    "v_smfmac_f32_16x16x64_bf16 v[40:43], v[12:15], a[48:55], v113\n"
    "ds_read_b128 v[64:67], v121 offset:2048\n"
    "v_smfmac_f32_16x16x64_bf16 v[32:35], v[12:15], a[32:39], v113\n"
    "ds_read_b128 v[68:71], v121 offset:6144\n"
    "v_smfmac_f32_16x16x64_bf16 v[44:47], v[12:15], a[56:63], v113\n"
    "ds_read_b128 v[72:75], v121 offset:3072\n"
    "v_smfmac_f32_16x16x64_bf16 v[36:39], v[12:15], a[40:47], v113\n"
    "ds_read_b128 v[76:79], v121 offset:7168\n"
    "s_waitcnt vmcnt(4)\n"
    "v_smfmac_f32_16x16x64_bf16 v[40:43], v[16:19], a[80:87], v113\n"
    "buffer_load_dwordx4 v112, s[8:11], s90 offen offset:1024 sc1 lds\n"
    "s_mov_b32 m0, s63\n"
    "v_smfmac_f32_16x16x64_bf16 v[32:35], v[16:19], a[64:71], v113\n"
    "v_smfmac_f32_16x16x64_bf16 v[44:47], v[16:19], a[88:95], v113\n"
    "v_smfmac_f32_16x16x64_bf16 v[36:39], v[16:19], a[72:79], v113\n"
    "v_mov_b32 v122, 0\n"
    "s_waitcnt vmcnt(4)\n"
    "v_smfmac_f32_16x16x64_bf16 v[40:43], v[20:23], a[112:119], v113\n"
    "buffer_load_dwordx4 v112, s[8:11], s59 offen offset:0 sc1 lds\n"
    "v_smfmac_f32_16x16x64_bf16 v[32:35], v[20:23], a[96:103], v113\n"
    "v_smfmac_f32_16x16x64_bf16 v[44:47], v[20:23], a[120:127], v113\n"
    "v_smfmac_f32_16x16x64_bf16 v[36:39], v[20:23], a[104:111], v113\n"
    "s_waitcnt vmcnt(4)\n"
    "v_smfmac_f32_16x16x64_bf16 v[40:43], v[24:27], a[144:151], v113\n"
    "buffer_load_dwordx4 v112, s[8:11], s59 offen offset:1024 sc1 lds\n"
    "v_smfmac_f32_16x16x64_bf16 v[32:35], v[24:27], a[128:135], v113\n"
    "v_smfmac_f32_16x16x64_bf16 v[44:47], v[24:27], a[152:159], v113\n"
    "v_smfmac_f32_16x16x64_bf16 v[36:39], v[24:27], a[136:143], v113\n"
    "s_waitcnt vmcnt(4)\n"
    "v_smfmac_f32_16x16x64_bf16 v[40:43], v[28:31], a[176:183], v113\n"
    "s_nop 2\n"
    "v_smfmac_f32_16x16x64_bf16 v[32:35], v[28:31], a[160:167], v113\n"
    "s_nop 2\n"
    "v_smfmac_f32_16x16x64_bf16 v[44:47], v[28:31], a[184:191], v113\n"
    "v_add_f32 v40, v40, v42\n"
    "v_smfmac_f32_16x16x64_bf16 v[36:39], v[28:31], a[168:175], v113\n"
    "v_add_f32 v41, v41, v43\n"
    "v_cvt_pk_bf16_f32 v100, v40, v41\n"
    "v_add_f32 v32, v32, v34\n"
    "v_add_f32 v33, v33, v35\n"
    "v_cvt_pk_bf16_f32 v101, v32, v33\n"
    "s_nop 0\n"
    "v_add_f32 v44, v44, v46\n"
    "v_add_f32 v45, v45, v47\n"
    "v_cvt_pk_bf16_f32 v102, v44, v45\n"
    "v_add_f32 v36, v36, v38\n"
    "v_add_f32 v37, v37, v39\n"
    "v_cvt_pk_bf16_f32 v103, v36, v37\n"
    "ds_write2_b64 v114, v[100:101], v[102:103] offset1:16\n"
    "v_cmp_u_f32 vcc, v36, v37\n"
    "v_smfmac_f32_16x16x64_bf16 v[104:107], v[0:3], a[192:199], v113\n"
    "v_smfmac_f32_16x16x64_bf16 v[108:111], v[0:3], a[200:207], v113\n"
    "v_smfmac_f32_16x16x64_bf16 v[104:107], v[4:7], a[208:215], v113\n"
    "v_smfmac_f32_16x16x64_bf16 v[108:111], v[4:7], a[216:223], v113\n"
    "s_cbranch_vccnz LH_nan_%=\n"
    "LH_cont_%=:\n"
    "v_smfmac_f32_16x16x64_bf16 v[104:107], v[8:11], a[224:231], v113\n"
    "s_waitcnt lgkmcnt(0)\n"
    "s_barrier\n"
    "ds_read_b64_tr_b16 v[32:33], v115 offset:0\n"
    "ds_read_b64_tr_b16 v[34:35], v115 offset:128\n"
    "s_memtime s[20:21]\n"
    "v_smfmac_f32_16x16x64_bf16 v[108:111], v[8:11], a[232:239], v113\n"
    "ds_read_b64_tr_b16 v[36:37], v115 offset:256\n"
    "ds_read_b64_tr_b16 v[38:39], v115 offset:384\n"
    "v_smfmac_f32_16x16x64_bf16 v[104:107], v[12:15], a[240:247], v113\n"
    "ds_read_b64_tr_b16 v[40:41], v115 offset:512\n"
    "ds_read_b64_tr_b16 v[42:43], v115 offset:640\n"
    "v_smfmac_f32_16x16x64_bf16 v[108:111], v[12:15], a[248:255], v113\n"
    "ds_read_b64_tr_b16 v[44:45], v115 offset:768\n"
    "ds_read_b64_tr_b16 v[46:47], v115 offset:896\n"
    "v_smfmac_f32_16x16x64_bf16 v[104:107], v[16:19], v[128:135], v113\n"
    "s_waitcnt lgkmcnt(6)\n"
    "v_mfma_f32_16x16x32_bf16 v[92:95], v[48:51], v[32:35], 0\n"
    "v_mfma_f32_16x16x32_bf16 v[96:99], v[52:55], v[32:35], 0\n"
    "v_mov_b32 v102, 0\n"
    "v_mov_b32 v101, s23\n"
    "v_mfma_f32_16x16x32_bf16 v[88:91], v[32:35], v[32:35], 0\n"
    "s_waitcnt lgkmcnt(0)\n"
    "s_add_u32 s39, s20, s26\n"
    "v_mfma_f32_16x16x32_bf16 v[92:95], v[56:59], v[36:39], v[92:95]\n"
    "s_and_b32 s39, s39, -2\n"
    "v_mov_b32 v100, s39\n"
    "v_mfma_f32_16x16x32_bf16 v[96:99], v[60:63], v[36:39], v[96:99]\n"
    "buffer_store_dwordx2 v[100:101], v118, s[8:11], s31 offen sc1\n"
    "v_mfma_f32_16x16x32_bf16 v[88:91], v[36:39], v[36:39], v[88:91]\n"
    "ds_read_b128 v[48:51], v119 offset:0\n"
    "v_mfma_f32_16x16x32_bf16 v[92:95], v[64:67], v[40:43], v[92:95]\n"
    "ds_read_b128 v[52:55], v119 offset:1024\n"
    "v_mfma_f32_16x16x32_bf16 v[96:99], v[68:71], v[40:43], v[96:99]\n"
    "v_mov_b32 v100, 0\n"
    "v_mfma_f32_16x16x32_bf16 v[88:91], v[40:43], v[40:43], v[88:91]\n"
    "ds_read_b128 v[56:59], v119 offset:2048\n"
    "v_mfma_f32_16x16x32_bf16 v[92:95], v[72:75], v[44:47], v[92:95]\n"
    "ds_read_b128 v[60:63], v119 offset:3072\n"
    "ds_read_b128 v[64:67], v119 offset:4096\n"
    "v_mfma_f32_16x16x32_bf16 v[96:99], v[76:79], v[44:47], v[96:99]\n"
    "s_nop 1\n"
    "ds_read_b128 v[68:71], v119 offset:5120\n"
    "v_mfma_f32_16x16x32_bf16 v[88:91], v[44:47], v[44:47], v[88:91]\n"
    "ds_read_b128 v[72:75], v119 offset:6144\n"
    "ds_read_b128 v[76:79], v119 offset:7168\n"
    "v_smfmac_f32_16x16x64_bf16 v[108:111], v[16:19], v[136:143], v113\n"
    "v_max3_f32 v40, v92, v93, v94\n"
    "v_smfmac_f32_16x16x64_bf16 v[104:107], v[20:23], v[144:151], v113\n"
    "v_max3_f32 v40, v40, v95, v96\n"
    "v_max3_f32 v41, v97, v98, v99\n"
    "s_nop 0\n"
    "v_max3_f32 v42, v40, v41, v88\n"
    "v_max_f32 v40, v42, v89\n"
    "v_max_f32 v41, v42, v89\n"
    "v_smfmac_f32_16x16x64_bf16 v[108:111], v[20:23], v[152:159], v113\n"
    "s_waitcnt lgkmcnt(0)\n"
    "v_permlane32_swap_b32 v40, v41\n"
    "v_max_f32 v42, v40, v41\n"
    "v_max_f32 v43, v40, v41\n"
    "v_smfmac_f32_16x16x64_bf16 v[104:107], v[24:27], v[48:55], v113\n"
    "s_nop 0\n"
    "v_permlane16_swap_b32 v42, v43\n"
    "v_max_f32 v42, v42, v43\n"
    "v_mul_f32 v42, s36, v42\n"
    "v_smfmac_f32_16x16x64_bf16 v[108:111], v[24:27], v[56:63], v113\n"
    "v_fma_f32 v88, v88, s34, v42\n"
    "v_smfmac_f32_16x16x64_bf16 v[104:107], v[28:31], v[64:71], v113\n"
    "v_fma_f32 v89, v89, s34, v42\n"
    "v_smfmac_f32_16x16x64_bf16 v[108:111], v[28:31], v[72:79], v113\n"
    "v_exp_f32 v88, v88\n"
    "v_exp_f32 v89, v89\n"
    "v_pk_fma_f32 v[92:93], v[92:93], s[94:95], v[42:43] op_sel_hi:[1,1,0]\n"
    "v_pk_fma_f32 v[94:95], v[94:95], s[94:95], v[42:43] op_sel_hi:[1,1,0]\n"
    "v_pk_fma_f32 v[96:97], v[96:97], s[94:95], v[42:43] op_sel_hi:[1,1,0]\n"
    "v_pk_fma_f32 v[98:99], v[98:99], s[94:95], v[42:43] op_sel_hi:[1,1,0]\n"
    "v_exp_f32 v92, v92\n"
    "v_pk_add_f32 v[104:105], v[104:105], v[106:107]\n"
    "v_pk_add_f32 v[108:109], v[108:109], v[110:111]\n"
    "v_cvt_pk_bf16_f32 v103, v88, v89\n"
    "v_cvt_pk_bf16_f32 v101, v104, v105\n"
    "v_cvt_pk_bf16_f32 v123, v108, v109\n"
    "v_mfma_f32_16x16x16_bf16 v[88:91], v[124:125], v[102:103], 0\n"
    "v_exp_f32 v93, v93\n"
    "v_mfma_f32_16x16x16_bf16 v[104:107], v[100:101], v[102:103], 0\n"
    "v_exp_f32 v94, v94\n"
    "v_mfma_f32_16x16x16_bf16 v[108:111], v[122:123], v[102:103], 0\n"
    "v_exp_f32 v95, v95\n"
    "v_exp_f32 v96, v96\n"
    "v_cvt_pk_bf16_f32 v44, v92, v93\n"
    "v_exp_f32 v97, v97\n"
    "v_cvt_pk_bf16_f32 v45, v94, v95\n"
    "v_exp_f32 v98, v98\n"
    "v_exp_f32 v99, v99\n"
    "v_cvt_pk_bf16_f32 v46, v96, v97\n"
    "v_cvt_pk_bf16_f32 v47, v98, v99\n"
    "v_mov_b32 v0, 0\n"
    "v_mov_b32 v1, 0\n"
    "v_mfma_f32_16x16x32_bf16 v[88:91], v[124:127], v[44:47], v[88:91]\n"
    "v_mov_b32 v2, 0\n"
    "v_mov_b32 v3, 0\n"
    "v_mfma_f32_16x16x32_bf16 v[104:107], v[80:83], v[44:47], v[104:107]\n"
    "v_mov_b32 v4, 0\n"
    "v_mov_b32 v5, 0\n"
    "v_mfma_f32_16x16x32_bf16 v[108:111], v[84:87], v[44:47], v[108:111]\n"
    "v_mov_b32 v6, 0\n"
    "v_mov_b32 v7, 0\n"
    "v_rcp_f32 v88, v88\n"
    "v_lshlrev_b32 v38, 1, v112\n"
    "v_and_b32 v38, 0x3e0, v38\n"
    "v_mul_f32 v104, v104, v88\n"
    "v_mul_f32 v105, v105, v88\n"
    "v_mul_f32 v106, v106, v88\n"
    "v_mul_f32 v107, v107, v88\n"
    "v_cvt_pk_bf16_f32 v32, v104, v105\n"
    "v_cvt_pk_bf16_f32 v33, v106, v107\n"
    "v_pk_mul_f32 v[108:109], v[108:109], v[88:89] op_sel_hi:[1,0]\n"
    "v_pk_mul_f32 v[110:111], v[110:111], v[88:89] op_sel_hi:[1,0]\n"
    "v_cvt_pk_bf16_f32 v34, v108, v109\n"
    "v_cvt_pk_bf16_f32 v35, v110, v111\n"
    "ds_write_b128 v116, v[32:35]\n"
    "buffer_load_dwordx3 v[40:42], v38, s[8:11], s31 offen sc1\n"
    "v_mov_b64_e64 v[8:9], 0\n"
    "v_mov_b64_e64 v[10:11], 0\n"
    "v_mov_b64_e64 v[12:13], 0\n"
    "v_mov_b64_e64 v[14:15], 0\n"
    "v_mov_b64 v[16:17], 0\n"
    "v_mov_b64 v[18:19], 0\n"
    "v_mov_b64 v[20:21], 0\n"
    "v_mov_b64 v[22:23], 0\n"
    "v_mov_b64 v[24:25], 0\n"
    "v_mov_b64 v[26:27], 0\n"
    "s_waitcnt lgkmcnt(0)\n"
    "s_barrier\n"
    "ds_read_b128 v[80:83], v112 offset:20992\n"
    "ds_read_b128 v[84:87], v112 offset:22016\n"
    "v_mov_b64 v[28:29], 0\n"
    "s_nop 1\n"
    "v_mov_b64 v[30:31], 0\n"
    "s_nop 1\n"
    "v_mov_b32 v39, 0\n"
    "s_nop 1\n"
    "s_waitcnt lgkmcnt(1)\n"
    "v_smfmac_f32_16x16x64_bf16 v[0:3], v[80:83], v[160:167], v113\n"
    "s_waitcnt lgkmcnt(0)\n"
    "v_smfmac_f32_16x16x64_bf16 v[0:3], v[84:87], v[224:231], v113\n"
    "ds_read_b128 v[48:51], v119 offset:32768\n"
    "v_smfmac_f32_16x16x64_bf16 v[4:7], v[80:83], v[168:175], v113\n"
    "ds_read_b128 v[52:55], v119 offset:33792\n"
    "v_smfmac_f32_16x16x64_bf16 v[4:7], v[84:87], v[232:239], v113\n"
    "ds_read_b128 v[56:59], v119 offset:34816\n"
    "s_nop 2\n"
    "v_add_f32 v0, v0, v2\n"
    "v_add_f32 v1, v1, v3\n"
    "v_smfmac_f32_16x16x64_bf16 v[8:11], v[80:83], v[176:183], v113\n"
    "ds_read_b128 v[60:63], v119 offset:35840\n"
    "v_cvt_pk_bf16_f32 v88, v0, v1\n"
    "v_smfmac_f32_16x16x64_bf16 v[8:11], v[84:87], v[240:247], v113\n"
    "ds_read_b128 v[64:67], v119 offset:36864\n"
    "v_add_f32 v4, v4, v6\n"
    "v_add_f32 v5, v5, v7\n"
    "v_cvt_pk_bf16_f32 v89, v4, v5\n"
    "buffer_store_dwordx2 v[88:89], v117, s[8:11], s29 offen sc1\n"
    "v_smfmac_f32_16x16x64_bf16 v[12:15], v[80:83], v[184:191], v113\n"
    "ds_read_b128 v[68:71], v119 offset:37888\n"
    "v_smfmac_f32_16x16x64_bf16 v[12:15], v[84:87], v[248:255], v113\n"
    "ds_read_b128 v[72:75], v119 offset:38912\n"
    "v_add_f32 v8, v8, v10\n"
    "v_add_f32 v9, v9, v11\n"
    "v_smfmac_f32_16x16x64_bf16 v[16:19], v[80:83], v[192:199], v113\n"
    "ds_read_b128 v[76:79], v119 offset:39936\n"
    "v_cvt_pk_bf16_f32 v90, v8, v9\n"
    "s_waitcnt lgkmcnt(4)\n"
    "v_smfmac_f32_16x16x64_bf16 v[16:19], v[84:87], v[48:55], v113\n"
    "s_add_u32 s24, s24, 1\n"
    "s_lshl_b32 s27, s24, 15\n"
    "v_add_f32 v12, v12, v14\n"
    "v_add_f32 v13, v13, v15\n"
    "v_cvt_pk_bf16_f32 v91, v12, v13\n"
    "buffer_store_dwordx2 v[90:91], v117, s[8:11], s30 offen sc1\n"
    "v_smfmac_f32_16x16x64_bf16 v[20:23], v[80:83], v[200:207], v113\n"
    "s_or_b32 s27, s27, s13\n"
    "s_and_b32 s39, s24, 16\n"
    "v_smfmac_f32_16x16x64_bf16 v[20:23], v[84:87], v[56:63], v113\n"
    "s_lshl_b32 s39, s39, 10\n"
    "s_xor_b32 s27, s27, s39\n"
    "v_add_f32 v16, v16, v18\n"
    "v_add_f32 v17, v17, v19\n"
    "v_smfmac_f32_16x16x64_bf16 v[24:27], v[80:83], v[208:215], v113\n"
    "s_mov_b32 s14, s65\n"
    "s_cmp_eq_u32 s24, 63\n"
    "v_cvt_pk_bf16_f32 v92, v16, v17\n"
    "s_waitcnt lgkmcnt(0)\n"
    "v_smfmac_f32_16x16x64_bf16 v[24:27], v[84:87], v[64:71], v113\n"
    "v_add_f32 v20, v20, v22\n"
    "v_add_f32 v21, v21, v23\n"
    "v_cvt_pk_bf16_f32 v93, v20, v21\n"
    "buffer_store_dwordx2 v[92:93], v117, s[8:11], s96 offen sc1\n"
    "v_smfmac_f32_16x16x64_bf16 v[28:31], v[80:83], v[216:223], v113\n"
    "v_smfmac_f32_16x16x64_bf16 v[28:31], v[84:87], v[72:79], v113\n"
    "s_nop 1\n"
    "v_add_f32 v24, v24, v26\n"
    "v_add_f32 v25, v25, v27\n"
    "v_cvt_pk_bf16_f32 v94, v24, v25\n"
    "s_nop 2\n"
    "v_add_f32 v28, v28, v30\n"
    "v_add_f32 v29, v29, v31\n"
    "v_cvt_pk_bf16_f32 v95, v28, v29\n"
    "buffer_store_dwordx2 v[94:95], v117, s[8:11], s97 offen sc1\n"
    "s_memtime s[82:83]\n"
    "s_cbranch_scc1 LH_end_%=\n"
    "s_waitcnt vmcnt(4)\n"
    "LH_cbchk_%=:\n"
    "v_cmp_eq_u32 vcc, s38, v40\n"
    "v_readlane_b32 s39, v42, s12\n"
    "v_subrev_u32 v40, s65, v40\n"
    "s_memtime s[56:57]\n"
    "s_memrealtime s[40:41]\n"
    "v_max_i32_dpp v41, v41, v41 row_shr:1 row_mask:0xf bank_mask:0xf\n"
    "v_max_i32_dpp v40, v40, v40 row_shr:1 row_mask:0xf bank_mask:0xf\n"
    "s_memrealtime s[42:43]\n"
    "v_max_i32_dpp v41, v41, v41 row_shr:2 row_mask:0xf bank_mask:0xf\n"
    "v_max_i32_dpp v40, v40, v40 row_shr:2 row_mask:0xf bank_mask:0xf\n"
    "s_memrealtime s[44:45]\n"
    "v_max_i32_dpp v41, v41, v41 row_shr:4 row_mask:0xf bank_mask:0xf\n"
    "v_max_i32_dpp v40, v40, v40 row_shr:4 row_mask:0xf bank_mask:0xf\n"
    "s_memrealtime s[46:47]\n"
    "v_max_i32_dpp v41, v41, v41 row_shr:8 row_mask:0xf bank_mask:0xf\n"
    "v_max_i32_dpp v40, v40, v40 row_shr:8 row_mask:0xf bank_mask:0xf\n"
    "s_memrealtime s[48:49]\n"
    "v_max_i32_dpp v41, v41, v41 row_bcast:15 row_mask:0xa bank_mask:0xf\n"
    "v_max_i32_dpp v40, v40, v40 row_bcast:15 row_mask:0xa bank_mask:0xf\n"
    "s_memrealtime s[50:51]\n"
    "v_readlane_b32 s3, v41, 31\n"
    "v_readlane_b32 s2, v40, 31\n"
    "s_memrealtime s[52:53]\n"
    "s_cmp_eq_u32 s39, s38\n"
    "s_cbranch_vccnz LH_fail3_%=\n"
    "s_memrealtime s[54:55]\n"
    "s_cbranch_scc1 LH_fail3_%=\n"
    "s_barrier\n"
    "s_add_i32 s37, s2, s91\n"
    "s_cmp_gt_i32 s2, s64\n"
    "s_cselect_b32 s37, s37, s18\n"
    "s_add_u32 s37, s37, s65\n"
    "s_add_u32 s65, s37, s58\n"
    "s_add_u32 s65, s65, s1\n"
    "s_sub_u32 s25, s65, 120\n"
    "s_waitcnt lgkmcnt(0)\n"
    "s_add_u32 s40, s40, s42\n"
    "s_add_u32 s44, s44, s46\n"
    "s_add_u32 s48, s48, s50\n"
    "s_add_u32 s52, s52, s54\n"
    "s_add_u32 s40, s40, s44\n"
    "s_add_u32 s48, s48, s52\n"
    "s_add_u32 s40, s40, s48\n"
    "s_mul_i32 s40, s40, 3\n"
    "s_sub_u32 s26, s40, s56\n"
    "s_cmp_gt_i32 s2, s64\n"
    "s_cbranch_scc1 LH_late_%=\n"
    "LH_idle2_%=:\n"
    "v_add_u32 v123, s63, v112\n"
    "ds_read_b128 v[80:83], v123\n"
    "ds_read_b128 v[84:87], v123 offset:1024\n"
    "s_sub_u32 s25, s25, s26\n"
    "s_add_u32 s6, s91, 1000\n"
    "s_min_u32 s6, s6, 0x2400\n"
    "s_lshr_b32 s7, s1, 3\n"
    "s_sub_u32 s7, s1, s7\n"
    "s_cmp_gt_i32 s2, s64\n"
    "s_cselect_b32 s6, s6, 2400\n"
    "s_cselect_b32 s0, 40, 0\n"
    "s_cselect_b32 s93, 4, 0\n"
    "s_max_u32 s7, s7, s0\n"
    "s_add_i32 s0, s3, s35\n"
    "s_add_i32 s0, s0, s7\n"
    "s_sub_i32 s0, s0, s18\n"
    "s_cmp_gt_i32 s0, 0x200\n"
    "s_cselect_b32 s0, s93, s0\n"
    "s_cmp_lt_u32 s24, 11\n"
    "s_cselect_b32 s39, 12, 2\n"
    "s_cmp_gt_i32 s0, 0\n"
    "s_cselect_b32 s39, 1, s39\n"
    "s_ashr_i32 s0, s0, s39\n"
    "s_mov_b32 s91, s6\n"
    "s_mov_b32 s1, s7\n"
    "s_add_u32 s18, s18, s0\n"
    "s_add_u32 s58, s58, s93\n"
    "s_add_u32 s35, s35, s93\n"
    "s_add_u32 s64, s64, s93\n"
    "LH_prep_%=:\n"
    "s_branch LH_top_%=\n"
    "LH_late_%=:\n"
    "s_sub_u32 s22, s65, 700\n"
    "LH_cw_%=:\n"
    "s_sleep 1\n"
    "s_memrealtime s[20:21]\n"
    "s_waitcnt lgkmcnt(0)\n"
    "s_mul_i32 s20, s20, 24\n"
    "s_sub_u32 s20, s22, s20\n"
    "s_sub_u32 s20, s20, 1\n"
    "s_cmp_lt_u32 s20, 0x4000\n"
    "s_cbranch_scc1 LH_cw_%=\n"
    "s_memtime s[56:57]\n"
    "s_memrealtime s[40:41]\n"
    "s_nop 1\n"
    "s_memrealtime s[42:43]\n"
    "s_nop 1\n"
    "s_memrealtime s[44:45]\n"
    "s_nop 1\n"
    "s_memrealtime s[46:47]\n"
    "s_nop 1\n"
    "s_memrealtime s[48:49]\n"
    "s_nop 1\n"
    "s_memrealtime s[50:51]\n"
    "s_nop 1\n"
    "s_memrealtime s[52:53]\n"
    "s_nop 1\n"
    "s_memrealtime s[54:55]\n"
    "s_waitcnt lgkmcnt(0)\n"
    "s_add_u32 s40, s40, s42\n"
    "s_add_u32 s44, s44, s46\n"
    "s_add_u32 s48, s48, s50\n"
    "s_add_u32 s52, s52, s54\n"
    "s_add_u32 s40, s40, s44\n"
    "s_add_u32 s48, s48, s52\n"
    "s_add_u32 s40, s40, s48\n"
    "s_mul_i32 s40, s40, 3\n"
    "s_sub_u32 s26, s40, s56\n"
    "s_branch LH_idle2_%=\n"
    "LH_nan_%=:\n"
    "v_max3_u32 v121, v0, v1, v2\n"
    "v_max3_u32 v121, v121, v3, v4\n"
    "v_max3_u32 v121, v121, v5, v6\n"
    "v_max3_u32 v121, v121, v7, v8\n"
    "v_max3_u32 v121, v121, v9, v10\n"
    "v_max3_u32 v121, v121, v11, v12\n"
    "v_max3_u32 v121, v121, v13, v14\n"
    "v_max3_u32 v121, v121, v15, v16\n"
    "v_max3_u32 v121, v121, v17, v18\n"
    "v_max3_u32 v121, v121, v19, v20\n"
    "v_max3_u32 v121, v121, v21, v22\n"
    "v_max3_u32 v121, v121, v23, v24\n"
    "v_max3_u32 v121, v121, v25, v26\n"
    "v_max3_u32 v121, v121, v27, v28\n"
    "v_max3_u32 v121, v121, v29, v30\n"
    "v_max_u32 v121, v121, v31\n"
    "v_cmp_eq_u32 vcc, s38, v121\n"
    "s_cbranch_vccz LH_cont_%=\n"
    "LH_fail1_%=:\n"
    "s_nop 7\n"
    "s_add_u32 s60, s60, 1\n"
    "s_add_u32 s33, s33, 1\n"
    "s_cmp_gt_u32 s33, 0x30d40\n"
    "s_cbranch_scc1 LH_end_%=\n"
    "s_branch LH_go_%=\n"
    "LH_fail3_%=:\n"
    "s_add_u32 s62, s62, 1\n"
    "s_add_u32 s33, s33, 1\n"
    "s_cmp_gt_u32 s33, 0x30d40\n"
    "s_cbranch_scc1 LH_end_%=\n"
    "s_sleep 8\n"
    "buffer_load_dwordx3 v[40:42], v38, s[8:11], s31 offen sc1\n"
    "s_waitcnt vmcnt(0)\n"
    "s_branch LH_cbchk_%=\n"
    "LH_end_%=:\n"
    "s_waitcnt vmcnt(0) lgkmcnt(0)\n"
    "s_endpgm\n"
        : "+{s16}"(a16), "+{s18}"(a18), "+{s58}"(a58), "+{s35}"(a35), "+{s64}"(a64), "+{s1}"(a1)
        : "{s[8:11]}"(rs), "{s12}"(a12), "{s13}"(a13), "{s15}"(a15), "{s17}"(a17), "{s19}"(a19), "{v[112:115]}"(c0), "{v[116:119]}"(c1), "{v120}"(c2), "{s31}"(a31), "{s84}"(a3),
          "{a[0:31]}"(W[0]), "{a[32:63]}"(W[1]), "{a[64:95]}"(W[2]), "{a[96:127]}"(W[3]), "{a[128:159]}"(W[4]), "{a[160:191]}"(W[5]), "{a[192:223]}"(W[6]), "{a[224:255]}"(W[7]),
          "{v[128:159]}"(W[8]), "{v[160:191]}"(W[9]), "{v[192:223]}"(W[10]), "{v[224:255]}"(W[11])
        : "v127", "s99", "vcc", "memory");
    __builtin_amdgcn_endpgm();
}
DI void role_red(Boot& bt, const int w, int h) {
    const Params p = params(bt);
    const int l = lane_id(), i = l & 15, g = l >> 4;
    const __amdgpu_buffer_rsrc_t rs = ws_rsrc(p);
    BST(64, 0);
    v4u m0[2] = {};
    if (h == 16) for (int k = 0; k < 2; ++k) { const int ch = 4 * k + ((l >> 4) & 1) + 2 * ((l >> 3) & 1), ml = (2 * w + (l >> 5)) * 8 + (l & 7), mi = ml & 15;
        const u16* m = p.mem0 + (2 * (mi >> 2) + (mi & 1)) * 512;
        for (int q = 0; q < 4; ++q) m0[k][q] = (u32)m[mem_col(ch, ml >> 4, (mi >> 1) & 1, 2 * q)] | ((u32)m[mem_col(ch, ml >> 4, (mi >> 1) & 1, 2 * q + 1)] << 16); }
    u32 lo = 0xffffffffu, hi = lo;
    if (h < 12 && w == 0) { boot_tag(rs, bt);
        for (int r = 0; r < NPROBE + 1; ++r) { const int c = (r + h) & 3; const u32 off = l < (r ? 2 : 8) ? (u32)(h >> 2) * (2u << 20) + (h & 3) * 32768 + (r ? c * 8192 : 0) + l * 4096 : OOB;
            unsigned long long t0, t1;
            asm volatile("s_memtime %0\n buffer_atomic_or %2, %3, %4, 0 offen sc1\n s_waitcnt vmcnt(0)\n s_memtime %1\n s_waitcnt lgkmcnt(0)" : "=&s"(t0), "=&s"(t1) : "v"(0), "v"(off), "s"(rs) : "memory");
            const u32 sh = (c & 1) * 16, cur = (c & 2) ? hi : lo, nw = (cur & ~(0xffffu << sh)) | (min((cur >> sh) & 0xffffu, (u32)(t1 - t0)) << sh);
            if (r) { if (c & 2) hi = nw; else lo = nw; } }
        st16c(rs, l == 0 ? 0 : OOB, O_PRB + (bt.b - 128) * 128, v4u{bt.ta, lo, bt.tb, hi}); }
    else __builtin_amdgcn_s_sleep(RSLP);
    wait_vm_bar<0>();
    boot_wait<0, true>(rs, bt, w, l);
    {
        const u32 t16 = w * 1024 + l * 16; const v4u sv = {SENT, SENT, SENT, SENT};
        for (int k = 0; k < 4; ++k) st16c(rs, t16, uni(B_PART + amap(h * 16384 + k * 4096, bt.s0)), sv);
        for (int k = 0; k < 4; ++k) { v4u o = sv;
            if (h == 16 && k < 2) o = m0[k];
            st16c(rs, t16, uni(B_MEM + amap((h ^ 16) * 16384 + k * 4096, bt.s1)), o); }
        if (w < 2) st16c(rs, l * 16, cb_off(2 * h + w, bt.s2), sv);
        if (w == 2 && h < 24) st16c(rs, l * 16, O_KMF + h * 1024, v4u{0, 0, 0, 0});
        boot_flag(rs, bt, w, l, O_RDY + h * 128); BST(64, 1);
    }
    if (RPREF & 1) touch<4, 8192>((const char*)p.Wo + (size_t)h * 131072 + w * 32768, l * 128);
    if (RPREF & 2) { const u32 vo = (l >> 1) * 8192 + (l & 1) * 128 + h * 256 + w * 128 * 8192;
        touch<1, 0>((const char*)p.Wq, vo); touch<1, 0>((const char*)p.Wk, vo); touch<1, 0>((const char*)p.Wv + 96 * 8192, vo); }
    if (CPREF && w == 1) { const char *hc, *rc; int ho, ro;
        asm volatile("s_getpc_b64 %0\n 1: s_mov_b64 %1, %0\n s_add_u32 vcc_lo, 0, Lhead_code - 1b\n s_add_u32 vcc_hi, 0, Lred_code - 1b\n s_mov_b32 %2, vcc_lo\n s_mov_b32 %3, vcc_hi" : "=&s"(hc), "=&s"(rc), "=&s"(ho), "=&s"(ro) :: "vcc", "scc");
        touch<1, 0>(hc + ho, min(l, 58) * 64); touch<1, 0>(rc + ro, min(l, 22) * 64); }
    __builtin_amdgcn_s_sleep(32);
    if (w == 0) { Boot b2 = bt; boot_wait<1, false>(rs, b2, w, l, O_RDY + (l & 31) * 128);
                  poll<6, true>(rs, 0, O_KF + (4 * h + 3) * 128, bt.ta, 1); }
    wait_vm_bar<0>();
    const u32 s0 = bt.s0, s1 = bt.s1, s2 = bt.s2;
    const u32 rw = 32 * w + h, mo = mem_ch(rw >> 4) + mem_l((rw & 15) * 4);
    v4u sel; v2u c0;
    c0.x = l * 16;
    c0.y = i < 4 ? mo + (2 * (i >> 1) + (g >> 1)) * 16 + (2 * (i & 1) + (g & 1)) * 4 : OOB;
    { const int js = (i & 2) ? 99 : 2 * (2 * ((i >> 2) & 1) + (i & 1)) + (i >> 3); const u32 one = (js & 1) ? 0x3f800000u : 0x3f80u;
      sel.x = (js >> 1) == 0 ? one : 0; sel.y = (js >> 1) == 1 ? one : 0; sel.z = (js >> 1) == 2 ? one : 0; sel.w = (js >> 1) == 3 ? one : 0; }
    u32 a12 = w * p.hb, a13 = B_MEM | (s1 << 13), a15 = B_CB | (s2 << 13), a18 = p.k1, a16 = O_KV + 2 * 524288 + h * 16384 + w * 4096, a58 = p.d2, a35 = p.mg, a64 = p.kexp, a1 = p.ka, a19 = O_DBG + (128 + h * 4 + w) * 8192,
        a23 = O_KMF + h * 256, c3 = (w == 3 && l == 0) ? 0 : OOB,
        a29 = uni(B_PART + amap(part_off(h, w), s0));
    asm volatile(
    "Lred_code:\n"
    "s_mov_b32 s24, 0\n"
    "s_mov_b32 s33, 0\n"
    "s_mov_b32 s60, 0\n"
    "s_mov_b32 s61, 0\n"
    "s_mov_b32 s62, 0\n"
    "s_mov_b32 s38, 0xffbfffbf\n"
    "v_mov_b32 v8, s38\n"
    "v_mov_b32 v9, s38\n"
    "v_mov_b32 v10, s38\n"
    "v_mov_b32 v11, s38\n"
    "v_lshlrev_b32 v2, 1, v0\n"
    "v_and_b32 v2, 0x3e0, v2\n"
    "v_lshlrev_b32 v29, 2, v0\n"
    "s_add_u32 s39, s24, 2\n"
    "s_bfe_u32 s31, s39, 0x30003\n"
    "s_lshl_b32 s31, s31, 15\n"
    "s_or_b32 s31, s31, s15\n"
    "s_and_b32 s39, s39, 7\n"
    "s_lshl_b32 s39, s39, 10\n"
    "s_or_b32 s31, s31, s39\n"
    "s_mov_b32 s24, 1\n"
    "s_lshl_b32 s27, s24, 15\n"
    "s_or_b32 s27, s27, s13\n"
    "s_and_b32 s39, s24, 16\n"
    "s_lshl_b32 s39, s39, 10\n"
    "s_xor_b32 s27, s27, s39\n"
    "s_memtime s[56:57]\n"
    "s_memrealtime s[40:41]\n"
    "s_nop 1\n"
    "s_memrealtime s[42:43]\n"
    "s_nop 1\n"
    "s_memrealtime s[44:45]\n"
    "s_nop 1\n"
    "s_memrealtime s[46:47]\n"
    "s_nop 1\n"
    "s_memrealtime s[48:49]\n"
    "s_nop 1\n"
    "s_memrealtime s[50:51]\n"
    "s_nop 1\n"
    "s_memrealtime s[52:53]\n"
    "s_nop 1\n"
    "s_memrealtime s[54:55]\n"
    "s_waitcnt lgkmcnt(0)\n"
    "s_add_u32 s40, s40, s42\n"
    "s_add_u32 s44, s44, s46\n"
    "s_add_u32 s48, s48, s50\n"
    "s_add_u32 s52, s52, s54\n"
    "s_add_u32 s40, s40, s44\n"
    "s_add_u32 s48, s48, s52\n"
    "s_add_u32 s40, s40, s48\n"
    "s_mul_i32 s40, s40, 3\n"
    "s_sub_u32 s26, s40, s56\n"
    "s_sub_u32 s65, s40, 0x10000\n"
    "s_mov_b32 s91, 2300\n"
    "s_mov_b32 s59, 1\n"
    "s_branch LR_ann_%=\n"
    "LR_top_%=:\n"
    "s_add_u32 s37, s37, s12\n"
    "s_cmp_eq_u32 s24, 63\n"
    "s_cselect_b32 s39, 0, 0x80000000\n"
    "v_or_b32 v30, s39, v3\n"
    "s_sub_u32 s22, s37, 1000\n"
    "LR_cw_%=:\n"
    "s_sleep 2\n"
    "s_memrealtime s[20:21]\n"
    "s_waitcnt lgkmcnt(0)\n"
    "s_mul_i32 s20, s20, 24\n"
    "s_sub_u32 s20, s22, s20\n"
    "s_sub_u32 s20, s20, 1\n"
    "s_cmp_lt_u32 s20, 0x4000\n"
    "s_cbranch_scc1 LR_cw_%=\n"
    "s_sub_u32 s22, s37, 580\n"
    "LR_wg_%=:\n"
    "s_memrealtime s[20:21]\n"
    "s_waitcnt lgkmcnt(0)\n"
    "s_mul_i32 s20, s20, 24\n"
    "s_sub_u32 s20, s22, s20\n"
    "s_sub_u32 s20, s20, 1\n"
    "s_cmp_lt_u32 s20, 0x4000\n"
    "s_cbranch_scc1 LR_wg_%=\n"
    "s_memtime s[56:57]\n"
    "s_memrealtime s[40:41]\n"
    "s_nop 1\n"
    "s_memrealtime s[42:43]\n"
    "s_nop 1\n"
    "s_memrealtime s[44:45]\n"
    "s_nop 1\n"
    "s_memrealtime s[46:47]\n"
    "s_nop 1\n"
    "s_memrealtime s[48:49]\n"
    "s_nop 1\n"
    "s_memrealtime s[50:51]\n"
    "s_nop 1\n"
    "s_memrealtime s[52:53]\n"
    "s_nop 1\n"
    "s_memrealtime s[54:55]\n"
    "s_sub_u32 s22, s37, 110\n"
    "s_waitcnt lgkmcnt(0)\n"
    "s_memtime s[20:21]\n"
    "s_add_u32 s40, s40, s42\n"
    "s_add_u32 s44, s44, s46\n"
    "s_add_u32 s48, s48, s50\n"
    "s_add_u32 s52, s52, s54\n"
    "s_add_u32 s40, s40, s44\n"
    "s_add_u32 s48, s48, s52\n"
    "s_add_u32 s40, s40, s48\n"
    "s_mul_i32 s40, s40, 3\n"
    "s_sub_u32 s26, s40, s56\n"
    "s_sub_u32 s22, s22, s26\n"
    "s_waitcnt lgkmcnt(0)\n"
    "s_sub_i32 s22, s22, s20\n"
    "s_ashr_i32 s22, s22, 3\n"
    "s_max_i32 s22, s22, 0\n"
    "s_min_i32 s22, s22, 63\n"
    "s_lshl_b32 s22, s22, 2\n"
    "s_getpc_b64 s[20:21]\n"
    "s_sub_u32 s22, 0x110, s22\n"
    "s_add_u32 s20, s20, s22\n"
    "s_addc_u32 s21, s21, 0\n"
    "s_setpc_b64 s[20:21]\n"
    ".rept 63\n"
    "s_nop 1\n"
    ".endr\n"
    "LR_pretry_%=:\n"
    "buffer_load_dwordx4 v[16:19], v0, s[8:11], s29 offen sc1\n"
    "buffer_load_dwordx4 v[20:23], v0, s[8:11], s29 offen offset:1024 sc1\n"
    "buffer_store_dword v8, v30, s[8:11], s23 offen offset:4 sc1\n"
    "v_mov_b32 v14, 0\n"
    "s_nop 2\n"
    "v_mov_b32 v14, 0\n"
    "s_nop 2\n"
    "v_mov_b32 v14, 0\n"
    "s_nop 2\n"
    "v_mov_b32 v14, 0\n"
    "s_nop 2\n"
    "v_mov_b32 v14, 0\n"
    "s_nop 2\n"
    "v_mov_b32 v14, 0\n"
    "s_nop 2\n"
    "v_mov_b32 v14, 0\n"
    "s_nop 2\n"
    "v_mov_b32 v14, 0\n"
    "s_nop 2\n"
    "v_mov_b32 v14, 0\n"
    "s_nop 2\n"
    "v_mov_b32 v14, 0\n"
    "s_nop 2\n"
    "v_mov_b32 v14, 0\n"
    "s_nop 2\n"
    "v_mov_b32 v14, 0\n"
    "s_nop 2\n"
    "v_mov_b32 v14, 0\n"
    "s_nop 2\n"
    "v_mov_b32 v14, 0\n"
    "s_nop 2\n"
    "v_mov_b32 v14, 0\n"
    "s_nop 2\n"
    "v_mov_b32 v14, 0\n"
    "s_nop 2\n"
    "v_mov_b32 v14, 0\n"
    "s_nop 2\n"
    "v_mov_b32 v14, 0\n"
    "s_nop 2\n"
    "v_mov_b32 v14, 0\n"
    "s_nop 2\n"
    "v_mov_b32 v14, 0\n"
    "s_nop 2\n"
    "v_mov_b32 v14, 0\n"
    "s_nop 2\n"
    "v_mov_b32 v14, 0\n"
    "s_nop 2\n"
    "s_waitcnt vmcnt(2)\n"
    "v_mfma_f32_16x16x32_bf16 v[24:27], v[4:7], v[16:19], 0\n"
    "v_max3_u32 v14, v16, v17, v18\n"
    "s_waitcnt vmcnt(1)\n"
    "v_mfma_f32_16x16x32_bf16 v[24:27], v[4:7], v[20:23], v[24:27]\n"
    "v_max3_u32 v14, v14, v19, v20\n"
    "v_max3_u32 v14, v14, v21, v22\n"
    "v_max_u32 v14, v14, v23\n"
    "v_cmp_eq_u32 vcc, s38, v14\n"
    "s_nop 3\n"
    "v_add_f32_dpp v24, v24, v24 row_ror:4 row_mask:0xf bank_mask:0xf\n"
    "v_add_f32_dpp v25, v25, v25 row_ror:4 row_mask:0xf bank_mask:0xf\n"
    "s_cbranch_vccnz LR_fail2_%=\n"
    "v_add_f32_dpp v24, v24, v24 row_ror:8 row_mask:0xf bank_mask:0xf\n"
    "v_add_f32_dpp v25, v25, v25 row_ror:8 row_mask:0xf bank_mask:0xf\n"
    "v_cvt_pk_bf16_f32 v24, v24, v25\n"
    "buffer_store_dword v24, v1, s[8:11], s27 offen sc1\n"
    "s_add_u32 s23, s23, 4\n"
    "s_cmp_ge_u32 s24, 63\n"
    "s_cbranch_scc1 LR_h1_%=\n"
    "s_sleep 4\n"
    "LR_h1_%=:\n"
    "buffer_store_dword v8, v3, s[8:11], s23 offen sc1\n"
    "s_cmp_eq_u32 s24, 63\n"
    "s_cbranch_scc1 LR_end_%=\n"
    "s_mov_b32 s59, 0\n"
    "s_sleep 14\n"
    "LR_ann_%=:\n"
    "buffer_load_dwordx2 v[12:13], v2, s[8:11], s31 offen sc1\n"
    "s_waitcnt vmcnt(0)\n"
    "v_cmp_eq_u32 vcc, s38, v12\n"
    "v_subrev_u32 v12, s65, v12\n"
    "s_cbranch_vccnz LR_fail3_%=\n"
    "v_max_i32_dpp v13, v13, v13 row_shr:1 row_mask:0xf bank_mask:0xf\n"
    "v_max_i32_dpp v12, v12, v12 row_shr:1 row_mask:0xf bank_mask:0xf\n"
    "s_nop 0\n"
    "v_max_i32_dpp v13, v13, v13 row_shr:2 row_mask:0xf bank_mask:0xf\n"
    "v_max_i32_dpp v12, v12, v12 row_shr:2 row_mask:0xf bank_mask:0xf\n"
    "s_nop 0\n"
    "v_max_i32_dpp v13, v13, v13 row_shr:4 row_mask:0xf bank_mask:0xf\n"
    "v_max_i32_dpp v12, v12, v12 row_shr:4 row_mask:0xf bank_mask:0xf\n"
    "s_nop 0\n"
    "v_max_i32_dpp v13, v13, v13 row_shr:8 row_mask:0xf bank_mask:0xf\n"
    "v_max_i32_dpp v12, v12, v12 row_shr:8 row_mask:0xf bank_mask:0xf\n"
    "s_nop 0\n"
    "v_max_i32_dpp v13, v13, v13 row_bcast:15 row_mask:0xa bank_mask:0xf\n"
    "v_max_i32_dpp v12, v12, v12 row_bcast:15 row_mask:0xa bank_mask:0xf\n"
    "v_readlane_b32 s3, v13, 31\n"
    "v_readlane_b32 s2, v12, 31\n"
    "s_nop 0\n"
    "s_add_i32 s37, s2, s91\n"
    "s_cmp_gt_i32 s2, s64\n"
    "s_cselect_b32 s37, s37, s18\n"
    "s_add_u32 s37, s37, s65\n"
    "s_cmp_eq_u32 s59, 1\n"
    "s_cbranch_scc1 LR_top_%=\n"
    "s_add_u32 s65, s37, s58\n"
    "s_add_u32 s65, s65, s1\n"
    "s_add_u32 s6, s91, 1000\n"
    "s_min_u32 s6, s6, 0x2400\n"
    "s_lshr_b32 s7, s1, 3\n"
    "s_sub_u32 s7, s1, s7\n"
    "s_cmp_gt_i32 s2, s64\n"
    "s_cselect_b32 s6, s6, 2400\n"
    "s_cselect_b32 s0, 40, 0\n"
    "s_cselect_b32 s93, 4, 0\n"
    "s_max_u32 s7, s7, s0\n"
    "s_add_i32 s0, s3, s35\n"
    "s_add_i32 s0, s0, s7\n"
    "s_sub_i32 s0, s0, s18\n"
    "s_cmp_gt_i32 s0, 0x200\n"
    "s_cselect_b32 s0, s93, s0\n"
    "s_cmp_lt_u32 s24, 11\n"
    "s_cselect_b32 s39, 12, 2\n"
    "s_cmp_gt_i32 s0, 0\n"
    "s_cselect_b32 s39, 1, s39\n"
    "s_ashr_i32 s0, s0, s39\n"
    "s_mov_b32 s91, s6\n"
    "s_mov_b32 s1, s7\n"
    "s_add_u32 s18, s18, s0\n"
    "s_add_u32 s58, s58, s93\n"
    "s_add_u32 s35, s35, s93\n"
    "s_add_u32 s64, s64, s93\n"
    "buffer_store_dwordx4 v[8:11], v0, s[8:11], s29 offen offset:0 sc1\n"
    "buffer_store_dwordx4 v[8:11], v0, s[8:11], s29 offen offset:1024 sc1\n"
    "s_add_u32 s37, s65, s18\n"
    "s_xor_b32 s29, s29, 0x100000\n"
    "s_add_u32 s39, s24, 2\n"
    "s_bfe_u32 s31, s39, 0x30003\n"
    "s_lshl_b32 s31, s31, 15\n"
    "s_or_b32 s31, s31, s15\n"
    "s_and_b32 s39, s39, 7\n"
    "s_lshl_b32 s39, s39, 10\n"
    "s_or_b32 s31, s31, s39\n"
    "s_add_u32 s24, s24, 1\n"
    "s_lshl_b32 s27, s24, 15\n"
    "s_or_b32 s27, s27, s13\n"
    "s_and_b32 s39, s24, 16\n"
    "s_lshl_b32 s39, s39, 10\n"
    "s_xor_b32 s27, s27, s39\n"
    "s_branch LR_top_%=\n"
    "LR_fail2_%=:\n"
    "s_add_u32 s61, s61, 1\n"
    "s_add_u32 s33, s33, 1\n"
    "s_cmp_gt_u32 s33, 0x30d40\n"
    "s_cbranch_scc1 LR_end_%=\n"
    "s_cmp_eq_u32 s59, 1\n"
    "s_cbranch_scc1 LR_f2b_%=\n"
    "s_bcnt1_i32_b64 s39, vcc\n"
    "s_add_u32 s39, s39, s59\n"
    "s_mov_b32 s59, 64\n"
    "s_cmp_le_u32 s39, 24\n"
    "s_cbranch_scc1 LR_pretry_%=\n"
    "s_mov_b32 s59, 1\n"
    "s_sleep 0\n"
    "s_branch LR_ann_%=\n"
    "LR_f2b_%=:\n"
    "s_sleep 2\n"
    "s_branch LR_pretry_%=\n"
    "LR_fail3_%=:\n"
    "s_add_u32 s62, s62, 1\n"
    "s_add_u32 s33, s33, 1\n"
    "s_cmp_gt_u32 s33, 0x30d40\n"
    "s_cbranch_scc1 LR_end_%=\n"
    "s_sleep 6\n"
    "s_branch LR_ann_%=\n"
    "LR_end_%=:\n"
    "s_waitcnt vmcnt(0) lgkmcnt(0)\n"
    "s_endpgm\n"
        : "+{s29}"(a29), "+{s16}"(a16), "+{s23}"(a23), "+{s18}"(a18), "+{s58}"(a58), "+{s35}"(a35), "+{s64}"(a64), "+{s1}"(a1)
        : "{s[8:11]}"(rs), "{s12}"(a12), "{s13}"(a13), "{s15}"(a15), "{s19}"(a19), "{v[0:1]}"(c0), "{v[4:7]}"(sel), "{v3}"(c3)
        : "v47", "s99", "vcc", "memory");
    __builtin_amdgcn_endpgm();
}

#define ROLE(name, call) __global__ void __launch_bounds__(256, 1) name##_k() { Boot bt; u32 wv; int a0, a1, a2; \
    asm volatile("L" #name ":" : "={s[0:1]}"(bt.ka), "={s2}"(bt.b), "={s3}"(wv), "={s5}"(bt.k), "={s6}"(a0), "={s7}"(a1), "={s8}"(a2) :: "s4", "v0", "v1", "v2", "v3", "v4", "v5", "v6", "v7", "v8", "v9", "v10", "v11", "v12", "v13", "v14", "v15", "v16", "v17", "v18", "v19", "v20", "v21", "v22", "v23", "v24", "v25", "v26", "v27", "v28", "v29", "v30", "v31", "v32", "v33", "v34", "v35", "v36", "v37", "v38", "v39", "v40", "v41", "v42", "v43", "v44", "v45", "v46", "v47", "v48", "v49", "v50", "v51", "v52", "v53", "v54", "v55", "v56", "v57", "v58", "v59", "v60", "v61", "v62", "v63", "v64", "v65", "v66", "v67", "v68", "v69", "v70", "v71", "v72", "v73", "v74", "v75", "v76", "v77", "v78", "v79", "v80", "v81", "v82", "v83", "v84", "v85", "v86", "v87", "v88", "v89", "v90", "v91", "v92", "v93", "v94", "v95", "v96", "v97", "v98", "v99", "v100", "v101", "v102", "v103", "v104", "v105", "v106", "v107", "v108", "v109", "v110", "v111", "v112", "v113", "v114", "v115", "v116", "v117", "v118", "v119", "v120", "v121", "v122", "v123", "v124", "v125", "v126", "v127", "v128", "v129", "v130", "v131", "v132", "v133", "v134", "v135", "v136", "v137", "v138", "v139", "v140", "v141", "v142", "v143", "v144", "v145", "v146", "v147", "v148", "v149", "v150", "v151", "v152", "v153", "v154", "v155", "v156", "v157", "v158", "v159", "v160", "v161", "v162", "v163", "v164", "v165", "v166", "v167", "v168", "v169", "v170", "v171", "v172", "v173", "v174", "v175", "v176", "v177", "v178", "v179", "v180", "v181", "v182", "v183", "v184", "v185", "v186", "v187", "v188", "v189", "v190", "v191", "v192", "v193", "v194", "v195", "v196", "v197", "v198", "v199", "v200", "v201", "v202", "v203", "v204", "v205", "v206", "v207", "v208", "v209", "v210", "v211", "v212", "v213", "v214", "v215", "v216", "v217", "v218", "v219", "v220", "v221", "v222", "v223", "v224", "v225", "v226", "v227", "v228", "v229", "v230", "v231", "v232", "v233", "v234", "v235", "v236", "v237", "v238", "v239", "v240", "v241", "v242", "v243", "v244", "v245", "v246", "v247", "v248", "v249", "v250", "v251", "v252", "v253", "v254", "v255", "a0", "a1", "a2", "a3", "a4", "a5", "a6", "a7", "a8", "a9", "a10", "a11", "a12", "a13", "a14", "a15", "a16", "a17", "a18", "a19", "a20", "a21", "a22", "a23", "a24", "a25", "a26", "a27", "a28", "a29", "a30", "a31", "a32", "a33", "a34", "a35", "a36", "a37", "a38", "a39", "a40", "a41", "a42", "a43", "a44", "a45", "a46", "a47", "a48", "a49", "a50", "a51", "a52", "a53", "a54", "a55", "a56", "a57", "a58", "a59", "a60", "a61", "a62", "a63", "a64", "a65", "a66", "a67", "a68", "a69", "a70", "a71", "a72", "a73", "a74", "a75", "a76", "a77", "a78", "a79", "a80", "a81", "a82", "a83", "a84", "a85", "a86", "a87", "a88", "a89", "a90", "a91", "a92", "a93", "a94", "a95", "a96", "a97", "a98", "a99", "a100", "a101", "a102", "a103", "a104", "a105", "a106", "a107", "a108", "a109", "a110", "a111", "a112", "a113", "a114", "a115", "a116", "a117", "a118", "a119", "a120", "a121", "a122", "a123", "a124", "a125", "a126", "a127", "a128", "a129", "a130", "a131", "a132", "a133", "a134", "a135", "a136", "a137", "a138", "a139", "a140", "a141", "a142", "a143", "a144", "a145", "a146", "a147", "a148", "a149", "a150", "a151", "a152", "a153", "a154", "a155", "a156", "a157", "a158", "a159", "a160", "a161", "a162", "a163", "a164", "a165", "a166", "a167", "a168", "a169", "a170", "a171", "a172", "a173", "a174", "a175", "a176", "a177", "a178", "a179", "a180", "a181", "a182", "a183", "a184", "a185", "a186", "a187", "a188", "a189", "a190", "a191", "a192", "a193", "a194", "a195", "a196", "a197", "a198", "a199", "a200", "a201", "a202", "a203", "a204", "a205", "a206", "a207", "a208", "a209", "a210", "a211", "a212", "a213", "a214", "a215", "a216", "a217", "a218", "a219", "a220", "a221", "a222", "a223", "a224", "a225", "a226", "a227", "a228", "a229", "a230", "a231", "a232", "a233", "a234", "a235", "a236", "a237", "a238", "a239", "a240", "a241", "a242", "a243", "a244", "a245", "a246", "a247", "a248", "a249", "a250", "a251", "a252", "a253", "a254", "a255", "s9", "s10", "s11", "s12", "s13", "s14", "s15", "s16", "s17", "s18", "s19", "s20", "s21", "s22", "s23", "s24", "s25", "s26", "s27", "s28", "s29", "s30", "s31", "s33", "s34", "s35", "s36", "s37", "s38", "s39", "s40", "s41", "s42", "s43", "s44", "s45", "s46", "s47", "s48", "s49", "s50", "s51", "s52", "s53", "s54", "s55", "s56", "s57", "s58", "s59", "s60", "s61", "s62", "s63", "s64", "s65", "s66", "s67", "s68", "s69", "s70", "s71", "s72", "s73", "s74", "s75", "s76", "s77", "s78", "s79", "s80", "s81", "s82", "s83", "s84", "s85", "s86", "s87", "s88", "s89", "s90", "s91", "s92", "s93", "s94", "s95", "s96", "s97", "s98", "s99", "vcc", "scc", "memory"); \
    const int w = wv & 3; call; }
ROLE(role_qkv, role_qkv(bt, w, a0, a1))
ROLE(role_f1, role_f1(bt, w, a0, a1))
ROLE(role_spare, role_spare(bt, w, a0))
ROLE(role_e, role_e(bt, w, a0, a1, a2))
ROLE(role_red, role_red(bt, w, a0))
ROLE(role_head, role_head(bt, w, a0, a1))
__global__ void __launch_bounds__(256, 1) main_k(Params) {
    unsigned long long ka = (unsigned long long)__builtin_amdgcn_kernarg_segment_ptr(); u32 bid = blockIdx.x, w = uni(threadIdx.x >> 6);
    asm volatile("Lmain_entry:\n s_load_dword s98, s[0:1], 0x0\n s_load_dword s99, s[0:1], 0x40" : "+{s[0:1]}"(ka), "+{s2}"(bid), "+{s3}"(w) :: "v0", "v1", "v2", "v3", "v4", "v5", "v6", "v7", "v8", "v9", "v10", "v11", "v12", "v13", "v14", "v15", "v16", "v17", "v18", "v19", "v20", "v21", "v22", "v23", "v24", "v25", "v26", "v27", "v28", "v29", "v30", "v31", "v32", "v33", "v34", "v35", "v36", "v37", "v38", "v39", "v40", "v41", "v42", "v43", "v44", "v45", "v46", "v47", "v48", "v49", "v50", "v51", "v52", "v53", "v54", "v55", "v56", "v57", "v58", "v59", "v60", "v61", "v62", "v63", "v64", "v65", "v66", "v67", "v68", "v69", "v70", "v71", "v72", "v73", "v74", "v75", "v76", "v77", "v78", "v79", "v80", "v81", "v82", "v83", "v84", "v85", "v86", "v87", "v88", "v89", "v90", "v91", "v92", "v93", "v94", "v95", "v96", "v97", "v98", "v99", "v100", "v101", "v102", "v103", "v104", "v105", "v106", "v107", "v108", "v109", "v110", "v111", "v112", "v113", "v114", "v115", "v116", "v117", "v118", "v119", "v120", "v121", "v122", "v123", "v124", "v125", "v126", "v127", "v128", "v129", "v130", "v131", "v132", "v133", "v134", "v135", "v136", "v137", "v138", "v139", "v140", "v141", "v142", "v143", "v144", "v145", "v146", "v147", "v148", "v149", "v150", "v151", "v152", "v153", "v154", "v155", "v156", "v157", "v158", "v159", "v160", "v161", "v162", "v163", "v164", "v165", "v166", "v167", "v168", "v169", "v170", "v171", "v172", "v173", "v174", "v175", "v176", "v177", "v178", "v179", "v180", "v181", "v182", "v183", "v184", "v185", "v186", "v187", "v188", "v189", "v190", "v191", "v192", "v193", "v194", "v195", "v196", "v197", "v198", "v199", "v200", "v201", "v202", "v203", "v204", "v205", "v206", "v207", "v208", "v209", "v210", "v211", "v212", "v213", "v214", "v215", "v216", "v217", "v218", "v219", "v220", "v221", "v222", "v223", "v224", "v225", "v226", "v227", "v228", "v229", "v230", "v231", "v232", "v233", "v234", "v235", "v236", "v237", "v238", "v239", "v240", "v241", "v242", "v243", "v244", "v245", "v246", "v247", "v248", "v249", "v250", "v251", "v252", "v253", "v254", "v255", "a0", "a1", "a2", "a3", "a4", "a5", "a6", "a7", "a8", "a9", "a10", "a11", "a12", "a13", "a14", "a15", "a16", "a17", "a18", "a19", "a20", "a21", "a22", "a23", "a24", "a25", "a26", "a27", "a28", "a29", "a30", "a31", "a32", "a33", "a34", "a35", "a36", "a37", "a38", "a39", "a40", "a41", "a42", "a43", "a44", "a45", "a46", "a47", "a48", "a49", "a50", "a51", "a52", "a53", "a54", "a55", "a56", "a57", "a58", "a59", "a60", "a61", "a62", "a63", "a64", "a65", "a66", "a67", "a68", "a69", "a70", "a71", "a72", "a73", "a74", "a75", "a76", "a77", "a78", "a79", "a80", "a81", "a82", "a83", "a84", "a85", "a86", "a87", "a88", "a89", "a90", "a91", "a92", "a93", "a94", "a95", "a96", "a97", "a98", "a99", "a100", "a101", "a102", "a103", "a104", "a105", "a106", "a107", "a108", "a109", "a110", "a111", "a112", "a113", "a114", "a115", "a116", "a117", "a118", "a119", "a120", "a121", "a122", "a123", "a124", "a125", "a126", "a127", "a128", "a129", "a130", "a131", "a132", "a133", "a134", "a135", "a136", "a137", "a138", "a139", "a140", "a141", "a142", "a143", "a144", "a145", "a146", "a147", "a148", "a149", "a150", "a151", "a152", "a153", "a154", "a155", "a156", "a157", "a158", "a159", "a160", "a161", "a162", "a163", "a164", "a165", "a166", "a167", "a168", "a169", "a170", "a171", "a172", "a173", "a174", "a175", "a176", "a177", "a178", "a179", "a180", "a181", "a182", "a183", "a184", "a185", "a186", "a187", "a188", "a189", "a190", "a191", "a192", "a193", "a194", "a195", "a196", "a197", "a198", "a199", "a200", "a201", "a202", "a203", "a204", "a205", "a206", "a207", "a208", "a209", "a210", "a211", "a212", "a213", "a214", "a215", "a216", "a217", "a218", "a219", "a220", "a221", "a222", "a223", "a224", "a225", "a226", "a227", "a228", "a229", "a230", "a231", "a232", "a233", "a234", "a235", "a236", "a237", "a238", "a239", "a240", "a241", "a242", "a243", "a244", "a245", "a246", "a247", "a248", "a249", "a250", "a251", "a252", "a253", "a254", "a255", "s4", "s5", "s6", "s7", "s8", "s9", "s10", "s11", "s12", "s13", "s14", "s15", "s16", "s17", "s18", "s19", "s20", "s21", "s22", "s23", "s24", "s25", "s26", "s27", "s28", "s29", "s30", "s31", "s33", "s34", "s35", "s36", "s37", "s38", "s39", "s40", "s41", "s42", "s43", "s44", "s45", "s46", "s47", "s48", "s49", "s50", "s51", "s52", "s53", "s54", "s55", "s56", "s57", "s58", "s59", "s60", "s61", "s62", "s63", "s64", "s65", "s66", "s67", "s68", "s69", "s70", "s71", "s72", "s73", "s74", "s75", "s76", "s77", "s78", "s79", "s80", "s81", "s82", "s83", "s84", "s85", "s86", "s87", "s88", "s89", "s90", "s91", "s92", "s93", "s94", "s95", "s96", "s97", "s98", "s99", "vcc", "scc", "memory");
    const unsigned long long te = 0;
    const int b = bid & 255, r = b >> 3; const bool first = bid < 256;
    const u32 k = first ? (__builtin_amdgcn_s_getreg(20 | (3 << 11)) - b + WRONGK * (b % 5 == 1)) & 7 : bid >> 17;
    if (bid == 248 && w == 0) { Boot bt; bt.ka = ka; const Params p = params(bt); const __amdgpu_buffer_rsrc_t rs = ws_rsrc(p); boot_tag(rs, bt);
        st16c(rs, lane_id() == 0 ? 0 : OOB, O_REC, v4u{bt.ta, 0, bt.tb, k}); }
    const int c0 = ((b + k) & 7) ^ CLSX, cls = c0 == SWP ? 7 : c0 == 7 ? SWP : c0;
    int role, a0, a1 = 0, a2 = 0;
    if (cls >= 4 && cls < 7) { const int q = (cls - 4) * 32 + r; role = 0; a0 = q / 3; a1 = q % 3; }
    else if (cls == 7) { role = r < 28 ? 1 : 2; a0 = r < 28 ? r >> 1 : r - 28; a1 = r & 1; }
    else if (cls & 1) { role = 3; a0 = r & 15; a1 = cls >> 1; a2 = r >> 4; }
    else { role = r >= 16 ? 4 : 5; a0 = (cls >> 1) * 16 + (r & 15); a1 = (u32)te * 24u; }
    asm volatile("s_cmp_eq_u32 s9, 5\n s_cbranch_scc1 Lrole_head\n s_cmp_eq_u32 s9, 0\n s_cbranch_scc1 Lrole_qkv\n s_cmp_eq_u32 s9, 1\n s_cbranch_scc1 Lrole_f1\n s_cmp_eq_u32 s9, 2\n s_cbranch_scc1 Lrole_spare\n"
                 "s_cmp_eq_u32 s9, 3\n s_cbranch_scc1 Lrole_e\n s_cmp_eq_u32 s9, 4\n s_cbranch_scc1 Lrole_red\n s_branch Lrole_head"
                 :: "{s[0:1]}"(ka), "{s2}"(b), "{s3}"(w), "{s5}"(k), "{s6}"(a0), "{s7}"(a1), "{s8}"(a2), "{s9}"(role) : "scc", "memory");
}
constexpr int g_k1 = 2960, g_d2 = 825, g_hb = 100, g_kexp = 1360, g_mg = -30, g_ka = 60;
extern "C" void kernel_launch(void* const* d_in, const int* in_sizes, int n_in, void* d_out, int out_size, void* d_ws, size_t ws_size, hipStream_t st) {
    Params p;
    p.x = (const u16*)d_in[0]; p.mem0 = (const u16*)d_in[1]; p.Wq = (const u16*)d_in[2]; p.Wk = (const u16*)d_in[3]; p.Wv = (const u16*)d_in[4]; p.Wo = (const u16*)d_in[5];
    p.Wg = (const u16*)d_in[8]; p.Wu = (const u16*)d_in[9]; p.Wd = (const u16*)d_in[10]; p.out = (u16*)d_out;
    p.ws = (char*)(((size_t)d_ws + (2u << 20) - 1) & ~(size_t)((2u << 20) - 1));
    p.wsbytes = O_END; p.k1 = g_k1; p.d2 = g_d2; p.hb = g_hb; p.kexp = g_kexp; p.mg = g_mg; p.ka = g_ka; p.pad = 0;
    (void)hipFuncSetAttribute((const void*)main_k, hipFuncAttributeMaxDynamicSharedMemorySize, 160 * 1024);
    main_k<<<256, 256, 160 * 1024, st>>>(p);
}
```
